```python
import torch

def _softmax(x, axis=-1):
    return torch.softmax(x, dim=axis)
def _gelu(x):
    return torch.nn.functional.gelu(x, approximate="tanh")
def _silu(x):
    return torch.nn.functional.silu(x)
def _sigmoid(x):
    return torch.sigmoid(x)
def _layernorm(x, axis=-1, eps=1e-5):
    m = x.mean(dim=axis, keepdim=True)
    v = x.var(dim=axis, keepdim=True, unbiased=False)
    return (x - m) / torch.sqrt(v + eps)
def _scatter_add(tgt, idx, src, axis=0):
    return tgt.clone().index_add_(axis, idx, src)
def _segment_sum(src, seg, n):
    out = torch.zeros((n,) + src.shape[1:], dtype=src.dtype)
    return out.index_add_(0, seg, src)
def _pad(x, pw):
    flat = tuple(v for pair in reversed(pw) for v in pair)
    return torch.nn.functional.pad(x, flat)

kernel_name = 'gated_mix__ssm_conv_h3__dense_activation_relu__prompt'

def setup_inputs():

    _w = (lambda a: (lambda t: t.to(torch.bfloat16) if t.is_floating_point() else t)(torch.as_tensor(a)))
    def _A_setup():
        B, S, D = 1, 2048, 512
        ks = 4
        return {
            "q": _w((lambda *s: torch.randn(*s, dtype=torch.bfloat16) * 0.05)(B, S, D)),
            "k": _w((lambda *s: torch.randn(*s, dtype=torch.bfloat16) * 0.8)(B, S, D)),
            "v": _w((lambda *s: torch.randn(*s, dtype=torch.bfloat16) * 0.8)(B, S, D)),
            "shift_k": _w((lambda *s: torch.randn(*s, dtype=torch.bfloat16) * 0.8)(D, ks) * 0.5),
            "ssm_k": _w((lambda *s: torch.randn(*s, dtype=torch.bfloat16) * 0.8)(D, S) * (1.0 / S ** 0.5)),
            "ssm_D": _w((lambda *s: torch.randn(*s, dtype=torch.bfloat16) * 0.8)(D)),
        }
    _A_in = _A_setup()

    def _B_setup():
        B, S, D, Dff = 1, 2048, 512, 1792
        return {
            "x": _w((lambda *s: torch.randn(*s, dtype=torch.bfloat16) * 0.05)(B, S, D)),
            "w1": _w((lambda *s: torch.randn(*s, dtype=torch.bfloat16) * 0.05)(D, Dff)),
            "w2": _w((lambda *s: torch.randn(*s, dtype=torch.bfloat16) * 0.05)(Dff, D)),
        }
    _B_in = _B_setup()

    return {
        "A_q": _A_in["q"],
        "A_k": _A_in["k"],
        "A_v": _A_in["v"],
        "A_shift_k": _A_in["shift_k"],
        "A_ssm_k": _A_in["ssm_k"],
        "A_ssm_D": _A_in["ssm_D"],
        "B_x": _B_in["x"],
        "B_w1": _B_in["w1"],
        "B_w2": _B_in["w2"],
        "gate_w": _w((lambda *s: torch.randn(*s, dtype=torch.bfloat16) * 0.05)(512) * 16.0),
    }

def reference(A_q, A_k, A_v, A_shift_k, A_ssm_k, A_ssm_D, B_x, B_w1, B_w2, gate_w):
    def _A_ref(q, k, v, shift_k, ssm_k, ssm_D):
        B, S, D = q.shape
        ks = shift_k.shape[-1]
        k_pad = _pad(k, ((0, 0), (ks - 1, 0), (0, 0)))
        k_shift = torch.zeros_like(k)
        for t in range(ks):
            k_shift = k_shift + k_pad[:, t:t + S, :] * shift_k[:, ks - 1 - t][None, None, :]
        kv = k_shift * v
        L = 2 * S
        kv_pad = _pad(kv, ((0, 0), (0, S), (0, 0)))
        kf_pad = _pad(ssm_k, ((0, 0), (0, S)))
        kv_f = (lambda x, axes=(-2,-1): torch.fft.rfft2(x.float(), dim=axes))(kv_pad[:, None, :, :], axes=(1, 2))
        kf_f = (lambda x, axes=(-2,-1): torch.fft.rfft2(x.float(), dim=axes))(kf_pad[:, None, :], axes=(1, 2))
        kf_f = (lambda x, *d: x.permute(*d))(kf_f, 1, 2, 0)
        y_f = kv_f * kf_f[None, :, :, :]
        y = (lambda x, s, axes=(-2,-1): torch.fft.irfft2(x, s=s, dim=axes).to(torch.bfloat16))(y_f, (1, L), axes=(1, 2))[:, 0, :S, :]
        y = y + kv * ssm_D[None, None, :]
        return q * y

    def _B_ref(x, w1, w2):
        h = torch.einsum("bsd,df->bsf", x, w1)
        zero = torch.zeros_like(h)
        h = torch.maximum(h, zero)
        return torch.einsum("bsf,fd->bsd", h, w2)

    x = A_q
    g = _sigmoid((x * gate_w).sum(-1, keepdims=True))
    a = _A_ref(x, A_k, A_v, A_shift_k, A_ssm_k, A_ssm_D)
    b = _B_ref(x, B_w1, B_w2)
    return g * a + (1 - g) * b
```

```cpp
#include <hip/hip_runtime.h>
#include <hip/hip_bf16.h>
typedef unsigned short u16;
__device__ __forceinline__ float bf2f(u16 x){ return __uint_as_float(((unsigned)x)<<16); }
__device__ __forceinline__ u16 f2bf(float f){ __hip_bfloat16 b = __float2bfloat16(f); return *reinterpret_cast<u16*>(&b); }
__device__ __forceinline__ float rbf(float f){ return bf2f(f2bf(f)); }
constexpr int S=2048, D=512, F=1792;

__global__ void k_kv(const u16* k, const u16* v, const u16* sk, u16* kvT){
  int idx = blockIdx.x*256+threadIdx.x; int s = idx / D, d = idx % D;
  float acc = 0.f;
  for (int t=0;t<4;++t){ int j=3-t; int ss=s-j; float kk = ss>=0 ? bf2f(k[ss*D+d]) : 0.f;
    float p = rbf(kk*bf2f(sk[d*4+j])); acc = rbf(acc+p); }
  kvT[d*S+s] = f2bf(acc*bf2f(v[idx]));
}
__global__ void k_conv(const u16* kvT, const u16* ssmk, const u16* ssmD, u16* y2){
  int idx = blockIdx.x*256+threadIdx.x; int d = idx / S, s = idx % S;
  float acc=0.f;
  for (int j=0;j<=s;++j) acc += bf2f(kvT[d*S+s-j])*bf2f(ssmk[d*S+j]);
  float y = rbf(acc);
  float t = rbf(bf2f(kvT[d*S+s])*bf2f(ssmD[d]));
  y2[s*D+d] = f2bf(y+t);
}
__global__ void k_gate(const u16* x, const u16* gw, float* g){
  int s = blockIdx.x; int l = threadIdx.x; float a=0.f;
  for (int d=l; d<D; d+=64) a += rbf(bf2f(x[s*D+d])*bf2f(gw[d]));
  for (int o=32;o>0;o>>=1) a += __shfl_xor(a,o);
  if (l==0){ float z = rbf(a); g[s] = rbf(1.f/(1.f+__expf(-z))); }
}
__global__ void k_gemm1(const u16* x, const u16* w1, u16* h){
  int idx = blockIdx.x*256+threadIdx.x; int s = idx / F, f = idx % F; float a=0.f;
  for (int d=0; d<D; ++d) a += bf2f(x[s*D+d])*bf2f(w1[d*F+f]);
  h[idx] = f2bf(fmaxf(rbf(a),0.f));
}
__global__ void k_gemm2(const u16* h, const u16* w2, const u16* x, const u16* y2, const float* g, u16* out){
  int idx = blockIdx.x*256+threadIdx.x; int s = idx / D, d = idx % D; float a=0.f;
  for (int f=0; f<F; ++f) a += bf2f(h[s*F+f])*bf2f(w2[f*D+d]);
  float b = rbf(a); float gg = g[s];
  float aa = rbf(bf2f(x[idx])*bf2f(y2[idx]));
  out[idx] = f2bf(rbf(gg*aa) + rbf(rbf(1.f-gg)*b));
}
extern "C" void kernel_launch(void* const* d_in, const int* in_sizes, int n_in, void* d_out, int out_size, void* d_ws, size_t ws_size, hipStream_t stream){
  const u16* q=(const u16*)d_in[0]; const u16* k=(const u16*)d_in[1]; const u16* v=(const u16*)d_in[2];
  const u16* sk=(const u16*)d_in[3]; const u16* ssmk=(const u16*)d_in[4]; const u16* ssmD=(const u16*)d_in[5];
  const u16* w1=(const u16*)d_in[7]; const u16* w2=(const u16*)d_in[8]; const u16* gw=(const u16*)d_in[9];
  char* ws=(char*)d_ws;
  u16* kvT=(u16*)ws; u16* y2=(u16*)(ws+(4<<20)); u16* h=(u16*)(ws+(8<<20)); float* g=(float*)(ws+(32<<20));
  k_kv<<<S*D/256,256,0,stream>>>(k,v,sk,kvT);
  k_conv<<<S*D/256,256,0,stream>>>(kvT,ssmk,ssmD,y2);
  k_gate<<<S,64,0,stream>>>(q,gw,g);
  k_gemm1<<<S*F/256,256,0,stream>>>(q,w1,h);
  k_gemm2<<<S*D/256,256,0,stream>>>(h,w2,q,y2,g,(u16*)d_out);
}
```

```cpp
#include <hip/hip_runtime.h>
#pragma clang diagnostic ignored "-Winline-asm"
typedef unsigned short u16; typedef unsigned u32; typedef unsigned long long u64;
typedef u32 u32x4 __attribute__((ext_vector_type(4)));
typedef u32 u32x2 __attribute__((ext_vector_type(2)));
typedef float f32x4 __attribute__((ext_vector_type(4)));
typedef float f32x2 __attribute__((ext_vector_type(2)));
typedef __bf16 bf16x8 __attribute__((ext_vector_type(8)));
typedef __bf16 bf16x2 __attribute__((ext_vector_type(2)));
typedef short s16x4 __attribute__((ext_vector_type(4)));
typedef short s16x2 __attribute__((ext_vector_type(2)));
typedef __amdgpu_buffer_rsrc_t rsrc_t;
#define LAS __attribute__((address_space(3)))
#define DEVI __device__ __forceinline__

constexpr u32 WS_G = 0, WS_F1 = 4096, WS_FH = 8192, WS_F3 = 12288, WS_GATE = 16384, WS_DBG = 65536;
constexpr u32 WS_UT = 1u << 20, WS_Y2 = 3u << 20, WS_W2F = 5u << 20, WS_HF = 7u << 20, WS_END = 14u << 20;
constexpr u32 L_W1 = 0, L_KC = 0, KC_CH = 34816, L_KV = 114688, L_W2S = 123904, L_R = 132096, R_CH = 4608,
              L_RED = 141312, L_RED2 = 0, L_Y2 = 114688, LDS_BYTES = 157696;
constexpr u32 SPIN_MAX = 200000;

DEVI rsrc_t mk(const void* p, u32 bytes) { return __builtin_amdgcn_make_buffer_rsrc((void*)p, (short)0, (int)bytes, 0x00020000); }
template <u32 C> DEVI u32 sc() { u32 r; asm volatile("s_mov_b32 %0, %1" : "=s"(r) : "n"(C)); return r; }
template <int OFF> DEVI void ld16(u32x4& d, u32 vo, rsrc_t r, u32 so) {
  asm volatile("buffer_load_dwordx4 %0, %1, %2, %3 offen offset:%4" : "=v"(d) : "v"(vo), "s"(r), "s"(so), "n"(OFF)); }
template <int OFF> DEVI void ld16c(u32x4& d, u32 vo, rsrc_t r, u32 so) {
  asm volatile("buffer_load_dwordx4 %0, %1, %2, %3 offen offset:%4 sc1" : "=v"(d) : "v"(vo), "s"(r), "s"(so), "n"(OFF)); }
template <int OFF> DEVI void ld16ca(u32x4& d, u32 vo, rsrc_t r, u32 so) {
  asm volatile("buffer_load_dwordx4 %0, %1, %2, %3 offen offset:%4 sc1" : "=a"(d) : "v"(vo), "s"(r), "s"(so), "n"(OFF)); }
template <int OFF> DEVI void ld8(u32x2& d, u32 vo, rsrc_t r, u32 so) {
  asm volatile("buffer_load_dwordx2 %0, %1, %2, %3 offen offset:%4" : "=v"(d) : "v"(vo), "s"(r), "s"(so), "n"(OFF)); }
DEVI void ld8c(u32x2& d, u32 vo, rsrc_t r, u32 so) {
  asm volatile("buffer_load_dwordx2 %0, %1, %2, %3 offen sc1" : "=v"(d) : "v"(vo), "s"(r), "s"(so)); }
DEVI void ld4c(u32& d, u32 vo, rsrc_t r, u32 so) {
  asm volatile("buffer_load_dword %0, %1, %2, %3 offen sc1" : "=v"(d) : "v"(vo), "s"(r), "s"(so)); }
DEVI void ld2(u32& d, u32 vo, rsrc_t r, u32 so) {
  asm volatile("buffer_load_ushort %0, %1, %2, %3 offen" : "=v"(d) : "v"(vo), "s"(r), "s"(so)); }
template <int OFF> DEVI void st16c(u32x4 d, u32 vo, rsrc_t r, u32 so) {
  asm volatile("buffer_store_dwordx4 %0, %1, %2, %3 offen offset:%4 sc1\n\ts_nop 1" :: "v"(d), "v"(vo), "s"(r), "s"(so), "n"(OFF) : "memory"); }
template <int OFF> DEVI void st16(u32x4 d, u32 vo, rsrc_t r, u32 so) {
  asm volatile("buffer_store_dwordx4 %0, %1, %2, %3 offen offset:%4\n\ts_nop 1" :: "v"(d), "v"(vo), "s"(r), "s"(so), "n"(OFF) : "memory"); }
template <int OFF> DEVI void st8c(u32x2 d, u32 vo, rsrc_t r, u32 so) {
  asm volatile("buffer_store_dwordx2 %0, %1, %2, %3 offen offset:%4 sc1" :: "v"(d), "v"(vo), "s"(r), "s"(so), "n"(OFF) : "memory"); }
DEVI void stflag(u32 e0, u32 e1, rsrc_t r, u32 so) {
  asm volatile("buffer_store_dwordx2 %0, %1, %2, %3 offen sc1 ; FLAGST" :: "v"(u32x2{e0, e1}), "v"(0u), "s"(r), "s"(so) : "memory"); }
DEVI void st4c(u32 d, u32 vo, rsrc_t r, u32 so) {
  asm volatile("buffer_store_dword %0, %1, %2, %3 offen sc1" :: "v"(d), "v"(vo), "s"(r), "s"(so) : "memory"); }
DEVI void dma16(u32 ldsbase, u32 vo, rsrc_t r, u32 so) {
  asm volatile("s_mov_b32 m0, %0\n\ts_nop 0\n\tbuffer_load_dwordx4 %1, %2, %3 offen lds" :: "s"(ldsbase), "v"(vo), "s"(r), "s"(so) : "m0", "memory"); }
template <int N> DEVI void waitv() { asm volatile("s_waitcnt vmcnt(%0)" :: "n"(N) : "memory"); }
template <int N> DEVI void waitv_bar() { asm volatile("s_waitcnt vmcnt(%0) lgkmcnt(0)\n\ts_barrier" :: "n"(N) : "memory"); }
DEVI void lbar() { asm volatile("s_waitcnt lgkmcnt(0)\n\ts_barrier" ::: "memory"); }
#define TIE1(a) asm volatile("" : "+v"(a))
#define TIE4(a, b, c, d) asm volatile("" : "+v"(a), "+v"(b), "+v"(c), "+v"(d))
#define TIE4A(a, b, c, d) asm volatile("" : "+a"(a), "+a"(b), "+a"(c), "+a"(d))

DEVI float blo(u32 x) { return __uint_as_float(x << 16); }
DEVI float bhi(u32 x) { return __uint_as_float(x & 0xffff0000u); }
DEVI u32 pk(float a, float b) { f32x2 f = {a, b}; return __builtin_bit_cast(u32, __builtin_convertvector(f, bf16x2)); }
DEVI f32x4 mfma(u32x4 a, u32x4 b, f32x4 c) {
  return __builtin_amdgcn_mfma_f32_16x16x32_bf16(__builtin_bit_cast(bf16x8, a), __builtin_bit_cast(bf16x8, b), c, 0, 0, 0); }
DEVI u32x2 trread(LAS char* p) { return __builtin_bit_cast(u32x2, __builtin_amdgcn_ds_read_tr16_b64_v4i16((LAS s16x4*)p)); }
DEVI u32 relu2(u32 x) { s16x2 v = __builtin_bit_cast(s16x2, x); s16x2 z = {0, 0}; return __builtin_bit_cast(u32, __builtin_elementwise_max(v, z)); }

DEVI void poll(u32 vo, rsrc_t r, u32 so, u32 e0, u32 e1) {
  for (u32 it = 0; it < SPIN_MAX; ++it) {
    u32x2 f; ld8c(f, vo, r, so); waitv<0>(); TIE1(f);
    if (__builtin_amdgcn_ballot_w64(f.x != e0 || f.y != e1) == 0ull) break;
    __builtin_amdgcn_s_sleep(1);
  }
}
#define STAMP(i) do {} while (0)

__global__ __launch_bounds__(256, 1) void gmix_kernel(const u16* __restrict__ q, const u16* __restrict__ kin, const u16* __restrict__ vin,
    const u16* __restrict__ shk, const u16* __restrict__ ssk, const u16* __restrict__ ssd, const u16* __restrict__ w1,
    const u16* __restrict__ w2, const u16* __restrict__ gw, u16* __restrict__ out, char* __restrict__ ws) {
  extern __shared__ __attribute__((aligned(16))) char smem[];
  LAS char* lds = (LAS char*)smem;
  const u32 lds0 = (u32)(size_t)lds;
  const u32 tid = threadIdx.x, l = tid & 63, r16 = l & 15, g = l >> 4;
  const u32 w = __builtin_amdgcn_readfirstlane(tid >> 6);
  const u32 bid = blockIdx.x, xcd = bid & 7, jb = bid >> 3;
  const u32 rp = 4 * (xcd >> 1) + (jb >> 3), ct = 8 * (xcd & 1) + (jb & 7);
  const u32 rt = 4 * xcd + (jb >> 3), cg = jb & 7;
  asm volatile("s_nop 4");
  const rsrc_t rWS = mk(ws, WS_END), rQ = mk(q, 2u << 20), rK = mk(kin, 2u << 20), rV = mk(vin, 2u << 20);
  STAMP(0);
  const rsrc_t rW1 = mk(w1, 512 * 1792 * 2);
  const u32 ch0 = 64 * cg + 2 * rt, Z = sc<0>();
  const u32 vone = tid ? 0x7fffff00u : 0u;

  u32x2 G; ld8c(G, 0, rWS, Z);
  const u32 c8 = tid & 7, rr = tid >> 3;
  u32x4 kr[5], vr[2], sk[4], gx[2], gwv;
  {
    const u32 vo = (64 * rt + 2 * rr) * 1024 + cg * 128 + c8 * 16;
    ld16<0>(kr[0], vo - 3072, rK, Z); ld16<0>(kr[1], vo - 2048, rK, Z); ld16<0>(kr[2], vo - 1024, rK, Z);
    ld16<0>(kr[3], vo, rK, Z); ld16<1024>(kr[4], vo, rK, Z);
    ld16<0>(vr[0], vo, rV, Z); ld16<1024>(vr[1], vo, rV, Z);
    const rsrc_t rS = mk(shk, 4096);
    const u32 so = cg * 512, v2 = c8 * 64;
    ld16<0>(sk[0], v2, rS, so); ld16<16>(sk[1], v2, rS, so); ld16<32>(sk[2], v2, rS, so); ld16<48>(sk[3], v2, rS, so);
    const u32 sg = (128 * rp + 8 * ct + 2 * w) * 1024;
    ld16<0>(gx[0], l * 16, rQ, sg); ld16<1024>(gx[1], l * 16, rQ, sg);
    ld16<0>(gwv, l * 16, mk(gw, 1024), Z);
  }
  const u32 w2ok = rt < 28;
  {
    const u32 phi = 2 * rt + (w >> 1), P = phi / 7, u = phi - 7 * P, t = w & 1;
    const u32 fb = (u < 6) ? (224 * P + 112 * (u / 3) + 32 * (u % 3) + 16 * t) : (224 * P + 96 + 112 * t);
    const rsrc_t rW2 = mk(w2, w2ok ? 1792 * 512 * 2 : 0);
    const u32 vo = (l >> 3) * 1024 + (l & 7) * 16, so = fb * 1024 + cg * 128;
    dma16(lds0 + L_W2S + w * 2048, vo, rW2, so);
    dma16(lds0 + L_W2S + w * 2048 + 1024, vo, rW2, so + 8192);
  }
  u32 vw1[7];
#pragma unroll
  for (int i = 0; i < 7; ++i) {
    const u32 o = 64 * (7 * w + i) + l, R = o / 14, c = o - 14 * R, rho = R & 31;
    const u32 k = (R & ~31u) + 8 * ((rho >> 2) & 3) + 4 * (rho >> 4) + (rho & 3);
    vw1[i] = k * 3584 + 16 * c;
  }
  const u32 vx0 = (32 * w + r16) * 1024 + 16 * g, vx1 = vx0 + 16384, sx = rp * 131072;
  u32x4 xf[16][2];
#define SS_LOAD(S) do { _Pragma("unroll") for (int i = 0; i < 7; ++i) \
      dma16(lds0 + L_W1 + (S) * 28672 + (7 * w + i) * 1024, vw1[i], rW1, (S) * 128 * 3584 + ct * 224); \
    ld16<(S) * 256 + 0>(xf[4 * (S) + 0][0], vx0, rQ, sx); ld16<(S) * 256 + 0>(xf[4 * (S) + 0][1], vx1, rQ, sx); \
    ld16<(S) * 256 + 64>(xf[4 * (S) + 1][0], vx0, rQ, sx); ld16<(S) * 256 + 64>(xf[4 * (S) + 1][1], vx1, rQ, sx); \
    ld16<(S) * 256 + 128>(xf[4 * (S) + 2][0], vx0, rQ, sx); ld16<(S) * 256 + 128>(xf[4 * (S) + 2][1], vx1, rQ, sx); \
    ld16<(S) * 256 + 192>(xf[4 * (S) + 3][0], vx0, rQ, sx); ld16<(S) * 256 + 192>(xf[4 * (S) + 3][1], vx1, rQ, sx); } while (0)
#define SS_TIE(S) do { TIE4(xf[4 * (S)][0], xf[4 * (S)][1], xf[4 * (S) + 1][0], xf[4 * (S) + 1][1]); \
    TIE4(xf[4 * (S) + 2][0], xf[4 * (S) + 2][1], xf[4 * (S) + 3][0], xf[4 * (S) + 3][1]); } while (0)
  SS_LOAD(0);
  SS_LOAD(1);

  waitv<30>();
  TIE1(G); TIE4(kr[0], kr[1], kr[2], kr[3]); TIE4(kr[4], vr[0], vr[1], gwv); TIE4(sk[0], sk[1], sk[2], sk[3]); TIE1(gx[0]); TIE1(gx[1]);
  STAMP(1);
  u32 e0, e1;
  { const u64 E = (((u64)G.y << 32) | G.x) + 1ull;
    e0 = __builtin_amdgcn_readfirstlane((u32)E); e1 = __builtin_amdgcn_readfirstlane((u32)(E >> 32)); }
  {
    u32x4 o0, o1;
#pragma unroll
    for (int m = 0; m < 4; ++m) {
      float r0[2], r1[2];
#pragma unroll
      for (int h = 0; h < 2; ++h) {
        const float a0 = blo(sk[m][2 * h]), a1 = bhi(sk[m][2 * h]), a2 = blo(sk[m][2 * h + 1]), a3 = bhi(sk[m][2 * h + 1]);
        float kk[5];
#pragma unroll
        for (int i = 0; i < 5; ++i) kk[i] = h ? bhi(kr[i][m]) : blo(kr[i][m]);
        const float v0 = h ? bhi(vr[0][m]) : blo(vr[0][m]), v1 = h ? bhi(vr[1][m]) : blo(vr[1][m]);
        r0[h] = (kk[0] * a3 + kk[1] * a2 + kk[2] * a1 + kk[3] * a0) * v0;
        r1[h] = (kk[1] * a3 + kk[2] * a2 + kk[3] * a1 + kk[4] * a0) * v1;
      }
      o0[m] = pk(r0[0], r0[1]); o1[m] = pk(r1[0], r1[1]);
    }
    *(LAS u32x4*)(lds + L_KV + (2 * rr) * 144 + c8 * 16) = o0;
    *(LAS u32x4*)(lds + L_KV + (2 * rr + 1) * 144 + c8 * 16) = o1;
  }
  float gval;
  {
    float p0 = 0.f, p1 = 0.f;
#pragma unroll
    for (int m = 0; m < 4; ++m) {
      p0 += blo(gx[0][m]) * blo(gwv[m]) + bhi(gx[0][m]) * bhi(gwv[m]);
      p1 += blo(gx[1][m]) * blo(gwv[m]) + bhi(gx[1][m]) * bhi(gwv[m]);
    }
    auto s = __builtin_amdgcn_permlane32_swap(__float_as_uint(p0), __float_as_uint(p1), false, false);
    float z = __uint_as_float(s[0]) + __uint_as_float(s[1]);
    auto s2 = __builtin_amdgcn_permlane16_swap(__float_as_uint(z), __float_as_uint(z), false, false);
    z = __uint_as_float(s2[0]) + __uint_as_float(s2[1]);
    z += __int_as_float(__builtin_amdgcn_update_dpp(0, __float_as_int(z), 0x128, 0xf, 0xf, false));
    z += __int_as_float(__builtin_amdgcn_update_dpp(0, __float_as_int(z), 0x124, 0xf, 0xf, false));
    z += __int_as_float(__builtin_amdgcn_update_dpp(0, __float_as_int(z), 0x122, 0xf, 0xf, false));
    z += __int_as_float(__builtin_amdgcn_update_dpp(0, __float_as_int(z), 0x121, 0xf, 0xf, false));
    z = blo(pk(z, z));
    gval = blo(pk(1.f / (1.f + __expf(-z)), 0.f));
  }
  lbar();
  {
    const u32 qq = r16 >> 2, pp = r16 & 3;
#pragma unroll
    for (int it = 0; it < 2; ++it) {
      const u32 m = 4 * it + g;
      LAS char* p = lds + L_KV + (63 - 8 * m - qq) * 144 + (16 * w + 4 * pp) * 2;
      const u32x2 a = trread(p), b = trread(p - 4 * 144);
      const u32x4 v = {a.x, a.y, b.x, b.y};
      st16c<0>(v, (16 * w + r16) * 4096 + 16 * m, rWS, WS_UT + cg * 64 * 4096 + (1984 - 64 * rt) * 2);
    }
    const rsrc_t rW = mk(ws, w2ok ? WS_END : 0);
#pragma unroll
    for (int ph = 0; ph < 2; ++ph) {
      LAS char* p = lds + L_W2S + ph * 4096 + (4 * g + qq) * 128 + (16 * w + 4 * pp) * 2;
      const u32x2 a = trread(p), b = trread(p + 2048);
      const u32x4 v = {a.x, a.y, b.x, b.y};
      st16c<0>(v, l * 16, rW, WS_W2F + ((cg * 56 + 2 * rt + ph) * 4 + w) * 1024);
    }
    st4c(__float_as_uint(gval), (l & 31) ? 0x7fffff00u : (l >> 5) * 4, rWS, WS_GATE + (128 * rp + 8 * ct + 2 * w) * 4);
  }
  u32x4 kc[2][2];
  {
    const rsrc_t rA = mk(ssk + ch0 * 2048, 4096), rB = mk(ssk + (ch0 + 1) * 2048, 4096);
    ld16<0>(kc[0][0], tid * 16 - 16, rA, Z); ld16<0>(kc[0][1], tid * 16, rA, Z);
    ld16<0>(kc[1][0], tid * 16 - 16, rB, Z); ld16<0>(kc[1][1], tid * 16, rB, Z);
  }
  SS_LOAD(2);
  STAMP(2);

  f32x4 acc1[7][2];
#pragma unroll
  for (int i = 0; i < 7; ++i) { acc1[i][0] = f32x4{0, 0, 0, 0}; acc1[i][1] = f32x4{0, 0, 0, 0}; }
  LAS char* pa = lds + L_W1 + (4 * g + (r16 >> 2)) * 224 + (r16 & 3) * 8;
#define SS_MM(S) do { _Pragma("unroll") for (int kb = 4 * (S); kb < 4 * (S) + 4; ++kb) { _Pragma("unroll") for (int ft = 0; ft < 7; ++ft) { \
      const u32x2 a_ = trread(pa + kb * 7168 + ft * 32), b_ = trread(pa + kb * 7168 + 3584 + ft * 32); \
      const u32x4 af = {a_.x, a_.y, b_.x, b_.y}; \
      acc1[ft][0] = mfma(af, xf[kb][0], acc1[ft][0]); acc1[ft][1] = mfma(af, xf[kb][1], acc1[ft][1]); } } } while (0)
#define KC_BUILD(CH) do { const u32x4 A_ = kc[CH][0], B_ = kc[CH][1]; const u32 d_[8] = {A_.x, A_.y, A_.z, A_.w, B_.x, B_.y, B_.z, B_.w}; \
    u32 o_[7]; _Pragma("unroll") for (int i = 0; i < 7; ++i) o_[i] = __builtin_amdgcn_alignbyte(d_[i + 1], d_[i], 2); \
    LAS char* pk_ = lds + L_KC + (CH) * KC_CH + (tid + 1) * 16; \
    *(LAS u32x4*)(pk_ + 16) = B_; \
    _Pragma("unroll") for (int c = 1; c < 8; ++c) { const int hb = c >> 1; \
      const u32x4 v_ = (c & 1) ? u32x4{o_[hb], o_[hb + 1], o_[hb + 2], o_[hb + 3]} : u32x4{d_[hb], d_[hb + 1], d_[hb + 2], d_[hb + 3]}; \
      *(LAS u32x4*)(pk_ + (272 * c + ((0xC0B6E590u >> (4 * c)) & 15)) * 16) = v_; } \
    if (tid < 8) *(LAS u32x4*)(lds + L_KC + (CH) * KC_CH + (272 * tid + ((0xC0B6E590u >> (4 * tid)) & 15) + (tid == 0)) * 16) = u32x4{0, 0, 0, 0}; } while (0)

  waitv_bar<39>(); SS_TIE(0);
  STAMP(3);
  SS_LOAD(3);
  SS_MM(0);
  waitv_bar<34>(); SS_TIE(1);
  st8c<0>(u32x2{e0, e1}, vone, rWS, WS_F1 + (cg * 32 + rt) * 8);
  if (tid < 60) *(LAS u32x4*)(lds + L_R + (tid >= 30 ? R_CH - 480 : 0) + 4096 + tid * 16) = u32x4{0, 0, 0, 0};
  SS_MM(1);
  waitv_bar<16>(); SS_TIE(2); TIE4(kc[0][0], kc[0][1], kc[1][0], kc[1][1]);
  STAMP(4);
  KC_BUILD(0);
  SS_MM(2);
  waitv_bar<1>(); SS_TIE(3);
  KC_BUILD(1);
  SS_MM(3);
  STAMP(5);

  poll((l & 31) * 8, rWS, WS_F1 + cg * 256, e0, e1);
  STAMP(6);
  u32x4 ur[2];
  ld16c<0>(ur[0], tid * 16, rWS, WS_UT + ch0 * 4096); ld16c<0>(ur[1], tid * 16, rWS, WS_UT + ch0 * 4096 + 4096);
  u32 dv; ld2(dv, 0, mk(ssd, 1024), (ch0 + (w >> 1)) * 2);
  {
    const u32 P = ct >> 1, par = ct & 1;
    const u32 sb = WS_HF + (((2 * rp + (w >> 1)) * 56 + 7 * P) * 4 + 2 * (w & 1)) * 1024;
#pragma unroll
    for (int st = 0; st < 2; ++st) {
#pragma unroll
      for (int u = 0; u < 3; ++u) {
        const f32x4 a = acc1[2 * u][st], b = acc1[2 * u + 1][st];
        const u32x4 v = {relu2(pk(a[0], a[1])), relu2(pk(a[2], a[3])), relu2(pk(b[0], b[1])), relu2(pk(b[2], b[3]))};
        st16c<0>(v, l * 16, rWS, sb + (3 * par + u) * 4096 + st * 1024);
      }
      const f32x4 a = acc1[6][st];
      st8c<0>(u32x2{relu2(pk(a[0], a[1])), relu2(pk(a[2], a[3]))}, l * 16, rWS, sb + 6 * 4096 + st * 1024 + par * 8);
    }
  }
  u32x4 wa[14][4];
  const u32 sw = WS_W2F + (cg * 56 + 14 * w) * 4096;
#define WA_LOAD(i) do { if ((i) < 12) { ld16ca<0>(wa[i][0], l * 16, rWS, sw + (i) * 4096); ld16ca<1024>(wa[i][1], l * 16, rWS, sw + (i) * 4096); \
    ld16ca<2048>(wa[i][2], l * 16, rWS, sw + (i) * 4096); ld16ca<3072>(wa[i][3], l * 16, rWS, sw + (i) * 4096); } else { \
    ld16c<0>(wa[i][0], l * 16, rWS, sw + (i) * 4096); ld16c<1024>(wa[i][1], l * 16, rWS, sw + (i) * 4096); \
    ld16c<2048>(wa[i][2], l * 16, rWS, sw + (i) * 4096); ld16c<3072>(wa[i][3], l * 16, rWS, sw + (i) * 4096); } } while (0)
#pragma unroll
  for (int i = 0; i < 10; ++i) WA_LOAD(i);
  waitv<48>(); TIE1(ur[0]); TIE1(ur[1]); TIE1(dv);
  *(LAS u32x4*)(lds + L_R + tid * 16) = ur[0];
  *(LAS u32x4*)(lds + L_R + R_CH + tid * 16) = ur[1];
  lbar();
  STAMP(7);

  f32x4 cacc[8];
#pragma unroll
  for (int i = 0; i < 8; ++i) cacc[i] = f32x4{0, 0, 0, 0};
  const u32 chw = w >> 1, hw = w & 1;
  {
    const u32 x0 = r16 + 1, c = x0 & 7;
    LAS char* pA = lds + L_KC + chw * KC_CH + (272 * c + ((0xC0B6E590u >> (4 * c)) & 15) + (x0 >> 3) + g + 16 * hw) * 16;
    LAS char* pB = lds + L_R + chw * R_CH + 4064 - 32 * r16 + 16 * g + 256 * hw - 3584;
#define CONV_R(r) do { u32x4 A_[8], B_[8]; \
    _Pragma("unroll") for (int i = 0; i < 8; ++i) { A_[i] = *(LAS u32x4*)(pA + (8 * i + (r)) * 64); B_[i] = *(LAS u32x4*)(pB + 3584 - 512 * i + 64 * (r)); } \
    _Pragma("unroll") for (int qq = 0; qq < 8; ++qq) _Pragma("unroll") for (int i = qq; i < 8; ++i) cacc[i] = mfma(A_[qq], B_[i - qq], cacc[i]); } while (0)
    CONV_R(0);
    waitv_bar<40>();
    st8c<0>(u32x2{e0, e1}, vone, rWS, WS_FH + (rp * 16 + ct) * 8);
#pragma unroll
    for (int i = 10; i < 14; ++i) WA_LOAD(i);
    CONV_R(1); CONV_R(2); CONV_R(3);
  }
  STAMP(8);
  {
    LAS char* pw = lds + L_RED + w * 4096 + l * 16;
    LAS char* pr = lds + L_RED + (w ^ 1) * 4096 + l * 16;
    f32x4 keep[4];
#pragma unroll
    for (int i = 0; i < 4; ++i) { *(LAS f32x4*)(pw + i * 1024) = hw ? cacc[i] : cacc[4 + i]; keep[i] = hw ? cacc[4 + i] : cacc[i]; }
    lbar();
    const float dd = blo(dv);
    u32x2 y[4];
#pragma unroll
    for (int i = 0; i < 4; ++i) {
      const f32x4 s = keep[i] + *(LAS f32x4*)(pr + i * 1024);
      const u32x2 uu = *(LAS u32x2*)(lds + L_R + chw * R_CH + (2044 - 1024 * hw - 256 * i - 16 * r16 - 4 * g) * 2);
      const u32 y01 = pk(s[0], s[1]), y23 = pk(s[2], s[3]);
      const u32 t01 = pk(bhi(uu.y) * dd, blo(uu.y) * dd), t23 = pk(bhi(uu.x) * dd, blo(uu.x) * dd);
      y[i].x = pk(blo(y01) + blo(t01), bhi(y01) + bhi(t01));
      y[i].y = pk(blo(y23) + blo(t23), bhi(y23) + bhi(t23));
    }
#pragma unroll
    for (int p = 0; p < 2; ++p) {
      auto s0 = __builtin_amdgcn_permlane16_swap(y[2 * p].x, y[2 * p + 1].x, false, false);
      auto s1 = __builtin_amdgcn_permlane16_swap(y[2 * p].y, y[2 * p + 1].y, false, false);
      const u32x4 v = {s0[0], s1[0], s0[1], s1[1]};
      st16c<0>(v, (256 * (g & 1) + 16 * r16 + 8 * (g >> 1)) * 2, rWS, WS_Y2 + (ch0 + chw) * 4096 + (1024 * hw + 512 * p) * 2);
    }
  }
  poll((l & 15) * 8, rWS, WS_FH + (rt >> 1) * 128, e0, e1);
  lbar();
  st8c<0>(u32x2{e0, e1}, vone, rWS, WS_F3 + (cg * 32 + rt) * 8);
  STAMP(9);

  u32x4 hb[14][4];
  const u32 sh = WS_HF + (rt * 56 + 14 * w) * 4096;
#define HB_LOAD(i) do { ld16c<0>(hb[i][0], l * 16, rWS, sh + (i) * 4096); ld16c<1024>(hb[i][1], l * 16, rWS, sh + (i) * 4096); \
    ld16c<2048>(hb[i][2], l * 16, rWS, sh + (i) * 4096); ld16c<3072>(hb[i][3], l * 16, rWS, sh + (i) * 4096); } while (0)
#pragma unroll
  for (int i = 0; i < 10; ++i) HB_LOAD(i);
  f32x4 acc2[4][4];
#pragma unroll
  for (int a = 0; a < 4; ++a)
#pragma unroll
    for (int b = 0; b < 4; ++b) acc2[a][b] = f32x4{0, 0, 0, 0};
#define G2_MM(i) do { TIE4(hb[i][0], hb[i][1], hb[i][2], hb[i][3]); if ((i) < 12) TIE4A(wa[i][0], wa[i][1], wa[i][2], wa[i][3]); else TIE4(wa[i][0], wa[i][1], wa[i][2], wa[i][3]); \
    _Pragma("unroll") for (int a = 0; a < 4; ++a) _Pragma("unroll") for (int b = 0; b < 4; ++b) acc2[a][b] = mfma(wa[i][a], hb[i][b], acc2[a][b]); \
    __builtin_amdgcn_sched_barrier(0); } while (0)
  waitv<36>(); G2_MM(0); HB_LOAD(10);
  waitv<36>(); G2_MM(1); HB_LOAD(11);
  waitv<36>(); G2_MM(2); HB_LOAD(12);
  waitv<36>(); G2_MM(3); HB_LOAD(13);
  u32x2 qv[4]; u32 gg;
  {
    const u32 vo = (16 * w + r16) * 1024 + 8 * g, so = rt * 65536 + cg * 128;
    ld8<0>(qv[0], vo, rQ, so); ld8<32>(qv[1], vo, rQ, so); ld8<64>(qv[2], vo, rQ, so); ld8<96>(qv[3], vo, rQ, so);
    ld4c(gg, r16 * 4, rWS, WS_GATE + (64 * rt + 16 * w) * 4);
  }
  waitv<41>(); G2_MM(4);
  waitv<37>(); G2_MM(5);
  waitv<33>(); G2_MM(6);
  waitv<29>(); G2_MM(7);
  waitv<25>(); G2_MM(8);
  waitv<21>(); G2_MM(9);
  waitv<17>(); G2_MM(10);
  waitv<13>(); G2_MM(11);
  waitv<9>(); G2_MM(12);
  waitv<5>(); G2_MM(13);
  STAMP(10);
  poll((l & 31) * 8, rWS, WS_F3 + cg * 256, e0, e1);
  TIE4(qv[0], qv[1], qv[2], qv[3]); TIE1(gg);
  u32x4 yt[2];
  {
    const u32 vo = (tid >> 3) * 4096 + (tid & 7) * 16, so = WS_Y2 + cg * 64 * 4096 + rt * 128;
    ld16c<0>(yt[0], vo, rWS, so); ld16c<0>(yt[1], vo, rWS, so + 32 * 4096);
  }
#pragma unroll
  for (int b = 0; b < 4; ++b)
    if (b != (int)w) {
#pragma unroll
      for (int a = 0; a < 4; ++a) *(LAS f32x4*)(lds + L_RED2 + ((w * 4 + b) * 4 + a) * 1024 + l * 16) = acc2[a][b];
    }
  f32x4 own[4];
#pragma unroll
  for (int a = 0; a < 4; ++a) own[a] = w == 0 ? acc2[a][0] : w == 1 ? acc2[a][1] : w == 2 ? acc2[a][2] : acc2[a][3];
  waitv<0>(); TIE1(yt[0]); TIE1(yt[1]);
  *(LAS u32x4*)(lds + L_Y2 + (tid >> 3) * 160 + (tid & 7) * 16) = yt[0];
  *(LAS u32x4*)(lds + L_Y2 + (32 + (tid >> 3)) * 160 + (tid & 7) * 16) = yt[1];
  lbar();
  {
    const float gf = __uint_as_float(gg), og = blo(pk(1.f - gf, 0.f));
    u32x2 o[4];
#pragma unroll
    for (int a = 0; a < 4; ++a) {
      f32x4 s = own[a];
#pragma unroll
      for (int k = 1; k < 4; ++k) s += *(LAS f32x4*)(lds + L_RED2 + ((((w + k) & 3) * 4 + w) * 4 + a) * 1024 + l * 16);
      const u32x2 y = trread(lds + L_Y2 + (16 * a + 4 * g + (r16 >> 2)) * 160 + (16 * w + 4 * (r16 & 3)) * 2);
      const u32 b01 = pk(s[0], s[1]), b23 = pk(s[2], s[3]);
      const u32 a01 = pk(blo(qv[a].x) * blo(y.x), bhi(qv[a].x) * bhi(y.x)), a23 = pk(blo(qv[a].y) * blo(y.y), bhi(qv[a].y) * bhi(y.y));
      const u32 c01 = pk(gf * blo(a01), gf * bhi(a01)), c23 = pk(gf * blo(a23), gf * bhi(a23));
      const u32 d01 = pk(og * blo(b01), og * bhi(b01)), d23 = pk(og * blo(b23), og * bhi(b23));
      o[a].x = pk(blo(c01) + blo(d01), bhi(c01) + bhi(d01));
      o[a].y = pk(blo(c23) + blo(d23), bhi(c23) + bhi(d23));
    }
    const rsrc_t rO = mk(out, 2u << 20);
#pragma unroll
    for (int p = 0; p < 2; ++p) {
      auto s0 = __builtin_amdgcn_permlane16_swap(o[2 * p].x, o[2 * p + 1].x, false, false);
      auto s1 = __builtin_amdgcn_permlane16_swap(o[2 * p].y, o[2 * p + 1].y, false, false);
      const u32x4 v = {s0[0], s1[0], s0[1], s1[1]};
      st16<0>(v, (16 * w + r16) * 1024 + 32 * (g & 1) + 16 * (g >> 1), rO, rt * 65536 + cg * 128 + p * 64);
    }
  }
  STAMP(11);
  if (bid == 0) {
    poll(tid * 8 + WS_F1, rWS, Z, e0, e1);
    lbar();
    st8c<0>(u32x2{e0, e1}, vone, rWS, Z);
  }
}

extern "C" void kernel_launch(void* const* d_in, const int* in_sizes, int n_in, void* d_out, int out_size, void* d_ws,
                              size_t ws_size, hipStream_t stream) {
  (void)hipFuncSetAttribute((const void*)gmix_kernel, hipFuncAttributeMaxDynamicSharedMemorySize, (int)LDS_BYTES);
  gmix_kernel<<<256, 256, LDS_BYTES, stream>>>((const u16*)d_in[0], (const u16*)d_in[1], (const u16*)d_in[2], (const u16*)d_in[3],
      (const u16*)d_in[4], (const u16*)d_in[5], (const u16*)d_in[7], (const u16*)d_in[8], (const u16*)d_in[9], (u16*)d_out, (char*)d_ws);
}
```

```cpp
#include <hip/hip_runtime.h>
#pragma clang diagnostic ignored "-Winline-asm"
typedef unsigned short u16; typedef unsigned u32; typedef unsigned long long u64;
typedef u32 u32x4 __attribute__((ext_vector_type(4)));
typedef u32 u32x2 __attribute__((ext_vector_type(2)));
typedef float f32x4 __attribute__((ext_vector_type(4)));
typedef float f32x2 __attribute__((ext_vector_type(2)));
typedef __bf16 bf16x2 __attribute__((ext_vector_type(2)));
typedef short s16x4 __attribute__((ext_vector_type(4)));
typedef short s16x2 __attribute__((ext_vector_type(2)));
typedef __amdgpu_buffer_rsrc_t rsrc_t;
#define LAS __attribute__((address_space(3)))
#define DEVI __device__ __forceinline__
#define SB() __builtin_amdgcn_sched_barrier(0)
#define UNR _Pragma("unroll")

constexpr u32 WS_F1 = 4096, WS_FH = 8192, WS_F3 = 12288, WS_GATE = 16384, WS_DBG = 65536;
constexpr u32 WS_UT = 1u << 20, WS_Y2 = 3u << 20, WS_W2F = 5u << 20, WS_HF = 7u << 20, WS_END = 14u << 20;
constexpr u32 SLOT = 32768, L_RING = 32768, L_KV = 0, L_W2S = 8192, L_KC = 0, KC_CH = 34816, L_R = 69632, R_CH = 4608,
              L_RED = 98304, L_RED2 = 0, L_Y2 = 131072, LDS_BYTES = 163840;
constexpr u32 SPIN_MAX = 200000;

DEVI rsrc_t mk(const void* p, u32 bytes) { return __builtin_amdgcn_make_buffer_rsrc((void*)p, (short)0, (int)bytes, 0x00020000); }
template <u32 C> DEVI u32 sc() { u32 r; asm volatile("s_mov_b32 %0, %1" : "=s"(r) : "n"(C)); return r; }
template <int OFF> DEVI void ld16(u32x4& d, u32 vo, rsrc_t r, u32 so) {
  asm volatile("buffer_load_dwordx4 %0, %1, %2, %3 offen offset:%4" : "=v"(d) : "v"(vo), "s"(r), "s"(so), "n"(OFF)); }
template <int OFF> DEVI void ld16c(u32x4& d, u32 vo, rsrc_t r, u32 so) {
  asm volatile("buffer_load_dwordx4 %0, %1, %2, %3 offen offset:%4 sc1" : "=v"(d) : "v"(vo), "s"(r), "s"(so), "n"(OFF)); }
template <int OFF> DEVI void ld16ca(u32x4& d, u32 vo, rsrc_t r, u32 so) {
  asm volatile("buffer_load_dwordx4 %0, %1, %2, %3 offen offset:%4 sc1" : "=a"(d) : "v"(vo), "s"(r), "s"(so), "n"(OFF)); }
template <int OFF> DEVI void ld8(u32x2& d, u32 vo, rsrc_t r, u32 so) {
  asm volatile("buffer_load_dwordx2 %0, %1, %2, %3 offen offset:%4" : "=v"(d) : "v"(vo), "s"(r), "s"(so), "n"(OFF)); }
DEVI void ld8c(u32x2& d, u32 vo, rsrc_t r, u32 so) {
  asm volatile("buffer_load_dwordx2 %0, %1, %2, %3 offen sc1" : "=v"(d) : "v"(vo), "s"(r), "s"(so)); }
DEVI void ld4c(u32& d, u32 vo, rsrc_t r, u32 so) {
  asm volatile("buffer_load_dword %0, %1, %2, %3 offen sc1" : "=v"(d) : "v"(vo), "s"(r), "s"(so)); }
DEVI void ld2(u32& d, u32 vo, rsrc_t r, u32 so) {
  asm volatile("buffer_load_ushort %0, %1, %2, %3 offen" : "=v"(d) : "v"(vo), "s"(r), "s"(so)); }
template <int OFF> DEVI void st16c(u32x4 d, u32 vo, rsrc_t r, u32 so) {
  asm volatile("buffer_store_dwordx4 %0, %1, %2, %3 offen offset:%4 sc1\n\ts_nop 1" :: "v"(d), "v"(vo), "s"(r), "s"(so), "n"(OFF) : "memory"); }
template <int OFF> DEVI void st16(u32x4 d, u32 vo, rsrc_t r, u32 so) {
  asm volatile("buffer_store_dwordx4 %0, %1, %2, %3 offen offset:%4\n\ts_nop 1" :: "v"(d), "v"(vo), "s"(r), "s"(so), "n"(OFF) : "memory"); }
template <int OFF> DEVI void st8c(u32x2 d, u32 vo, rsrc_t r, u32 so) {
  asm volatile("buffer_store_dwordx2 %0, %1, %2, %3 offen offset:%4 sc1" :: "v"(d), "v"(vo), "s"(r), "s"(so), "n"(OFF) : "memory"); }
DEVI void st4c(u32 d, u32 vo, rsrc_t r, u32 so) {
  asm volatile("buffer_store_dword %0, %1, %2, %3 offen sc1" :: "v"(d), "v"(vo), "s"(r), "s"(so) : "memory"); }
template <int TAG> DEVI void dma16(u32 ldsbase, u32 vo, rsrc_t r, u32 so) {
  asm volatile("s_mov_b32 m0, %0\n\ts_nop 0\n\tbuffer_load_dwordx4 %1, %2, %3 offen lds ; TAG%4" :: "s"(ldsbase), "v"(vo), "s"(r), "s"(so), "n"(TAG) : "m0"); }
DEVI void setm0(u32 base, u32 add) { asm volatile("s_add_u32 m0, %0, %1 ; SETM0" :: "s"(base), "n"(add) : "m0", "scc"); }
template <int OFF, int TAG> DEVI void dmao(u32 vo, rsrc_t r, u32 so) {
  asm volatile("buffer_load_dwordx4 %0, %1, %2 offen offset:%3 lds ; TAG%4" :: "v"(vo), "s"(r), "s"(so), "n"(OFF), "n"(TAG)); }
template <int N> DEVI void waitv() { asm volatile("s_waitcnt vmcnt(%0)" :: "n"(N) : "memory"); }
template <int N, int TAG> DEVI void waitv_bar() { asm volatile("s_waitcnt vmcnt(%0) lgkmcnt(0) ; NEED%1\n\ts_barrier" :: "n"(N), "n"(TAG) : "memory"); }
DEVI void lbar() { asm volatile("s_waitcnt lgkmcnt(0)\n\ts_barrier" ::: "memory"); }
#define TIE1(a) asm volatile("" : "+v"(a))
#define TIE4(a, b, c, d) asm volatile("" : "+v"(a), "+v"(b), "+v"(c), "+v"(d))
#define TIE4A(a, b, c, d) asm volatile("" : "+a"(a), "+a"(b), "+a"(c), "+a"(d))

DEVI float blo(u32 x) { return __uint_as_float(x << 16); }
DEVI float bhi(u32 x) { return __uint_as_float(x & 0xffff0000u); }
DEVI u32 pk(float a, float b) { f32x2 f = {a, b}; return __builtin_bit_cast(u32, __builtin_convertvector(f, bf16x2)); }
DEVI f32x2 up(u32 x) { return f32x2{blo(x), bhi(x)}; }
DEVI u32 pk2(f32x2 f) { return __builtin_bit_cast(u32, __builtin_convertvector(f, bf16x2)); }
DEVI f32x2 rnd(f32x2 f) { return f32x2{__uint_as_float(pk(0.f, f[0])), __uint_as_float(pk(0.f, f[1]))}; }
template <bool FIRST> DEVI void mf(f32x4& c, const u32x4& a, const u32x4& b) {
  if (FIRST) asm volatile("v_mfma_f32_16x16x32_bf16 %0, %1, %2, 0" : "=&v"(c) : "v"(a), "v"(b));
  else asm volatile("v_mfma_f32_16x16x32_bf16 %0, %1, %2, %0" : "+v"(c) : "v"(a), "v"(b)); }
template <bool FIRST> DEVI void mfa(f32x4& c, const u32x4& a, const u32x4& b) {
  if (FIRST) asm volatile("v_mfma_f32_16x16x32_bf16 %0, %1, %2, 0" : "=&v"(c) : "a"(a), "v"(b));
  else asm volatile("v_mfma_f32_16x16x32_bf16 %0, %1, %2, %0" : "+v"(c) : "a"(a), "v"(b)); }
DEVI void mfdone() { asm volatile("s_nop 7\n\ts_nop 3"); }
DEVI u32x2 trread(LAS char* p) { return __builtin_bit_cast(u32x2, __builtin_amdgcn_ds_read_tr16_b64_v4i16((LAS s16x4*)p)); }
DEVI u32x4 trfrag(LAS char* p, u32 d) { const u32x2 a = trread(p), b = trread(p + d); return u32x4{a.x, a.y, b.x, b.y}; }
DEVI u32 relu2(u32 x) { s16x2 v = __builtin_bit_cast(s16x2, x); s16x2 z = {0, 0}; return __builtin_bit_cast(u32, __builtin_elementwise_max(v, z)); }
DEVI LAS char* opq(LAS char* p) { asm volatile("" : "+v"(p)); return p; }

DEVI void poll(u32x2 f, u32 vo, rsrc_t r, u32 so, u32 e0, u32 e1) {
  TIE1(f);
#pragma nounroll
  for (u32 it = 0; it < SPIN_MAX; ++it) {
    if (__builtin_amdgcn_ballot_w64(f.x != e0 || f.y != e1) == 0ull) break;
    __builtin_amdgcn_s_sleep(1);
    asm volatile("buffer_load_dwordx2 %0, %1, %2, %3 offen sc1 ; FLAGST\n\ts_waitcnt vmcnt(0) ; FLAGST" : "=v"(f) : "v"(vo), "s"(r), "s"(so) : "memory");
  }
}
#define STAMP(i) do {} while (0)
#define STAMPC(i) do {} while (0)

__global__ __launch_bounds__(256, 1) void gmix_kernel(const u16* __restrict__ q, const u16* __restrict__ kin, const u16* __restrict__ vin,
    const u16* __restrict__ shk, const u16* __restrict__ ssk, const u16* __restrict__ ssd, const u16* __restrict__ w1,
    const u16* __restrict__ w2, const u16* __restrict__ gw, u16* __restrict__ out, char* __restrict__ ws) {
  extern __shared__ __attribute__((aligned(16))) char smem[];
  u32 pfd;
  {
    u64 pc; u32 len;
    asm volatile("s_getpc_b64 %0\nLgm_pc:\n\ts_mov_b32 %1, Lgm_end-Lgm_pc" : "=s"(pc), "=s"(len));
    asm volatile("buffer_load_dword %0, %1, %2, 0 offen" : "=v"(pfd) : "v"((((blockIdx.x >> 3) & 3) * 64 + (threadIdx.x >> 2)) * 128), "s"(mk((const void*)pc, len)));
  }
  LAS char* lds = (LAS char*)smem;
  const u32 lds0 = (u32)(size_t)lds;
  const u32 tid = threadIdx.x, l = tid & 63, r16 = l & 15, g = l >> 4;
  const u32 w = __builtin_amdgcn_readfirstlane(tid >> 6);
  const u32 bid = blockIdx.x, xcd = bid & 7, jb = bid >> 3;
  const u32 ja = (jb * 37) >> 8, jc = jb - 7 * ja, g1ok = jb < 28;
  const u32 rp = 4 * (xcd >> 1) + ja, ct = 7 * (xcd & 1) + jc;
  const u32 rt = 4 * xcd + (jb >> 3), cg = jb & 7;
  const rsrc_t rWS = mk(ws, WS_END), rQ = mk(q, 2u << 20), rK = mk(kin, 2u << 20), rV = mk(vin, 2u << 20);
  STAMP(0); STAMPC(14);
  const rsrc_t rW1 = mk(w1, g1ok ? 512 * 1792 * 2 : 0), rX = mk(q, g1ok ? 2u << 20 : 0);
  const u32 ch0 = 64 * cg + 2 * rt, Z = sc<0>();
  const u32 vone = tid ? 0x7fffff00u : 0u;

  u32x2 G; ld8c(G, l == 1 && bid < 40 ? WS_F1 + (bid * 4 + w) * 128 : 0, rWS, Z);
  const u32 c8 = tid & 7, rr = tid >> 3;
  u32x4 kr[5], vr[2], sk[4];
  {
    const u32 vo = (64 * rt + 2 * rr) * 1024 + cg * 128 + c8 * 16;
    ld16<0>(kr[0], vo - 3072, rK, Z); ld16<0>(kr[1], vo - 2048, rK, Z); ld16<0>(kr[2], vo - 1024, rK, Z);
    ld16<0>(kr[3], vo, rK, Z); ld16<1024>(kr[4], vo, rK, Z);
    ld16<0>(vr[0], vo, rV, Z); ld16<1024>(vr[1], vo, rV, Z);
    const rsrc_t rS = mk(shk, 4096);
    const u32 so = cg * 512, v2 = c8 * 64;
    ld16<0>(sk[0], v2, rS, so); ld16<16>(sk[1], v2, rS, so); ld16<32>(sk[2], v2, rS, so); ld16<48>(sk[3], v2, rS, so);
  }
  const u32 w2ok = rt < 28;
  {
    const u32 fb = 64 * rt + 16 * w;
    const rsrc_t rW2 = mk(w2, w2ok ? 1792 * 512 * 2 : 0);
    const u32 vo = (l >> 3) * 1024 + (l & 7) * 16, so = fb * 1024 + cg * 128;
    dma16<9>(lds0 + L_W2S + w * 2048, vo, rW2, so);
    dma16<9>(lds0 + L_W2S + w * 2048 + 1024, vo, rW2, so + 8192);
  }
  u32 vx[4], vw[4];
  UNR for (int i = 0; i < 4; ++i) {
    vx[i] = (l >> 3) * 1024 + (((l & 7) ^ g ^ (4 * (i & 1))) * 16) + i * (8192 - 1024);
    vw[i] = g * 3584 + ((r16 ^ (2 * g) ^ (8 * (i & 1))) * 16) + i * (28672 - 1024);
  }
  const u32 rot = (jc + 2 * ja) & 7;
  const u32 sx0 = (128 * rp + 32 * w) * 1024, sw0 = (32 * (w >> 1) + 4 * (w & 1)) * 3584 + ct * 256;
  const u32 lr0 = lds0 + L_RING + w * 4096;
#define SM0(S, h) setm0(lr0, ((S) & 3) * SLOT + (h) * 16384)
#define SDMA(S, p) do { const u32 ks_ = ((S) + rot) & 7; \
    if ((p) & 4) dmao<((p) & 3) * 1024, S>(vw[(p) & 3], rW1, sw0 + ks_ * 229376); \
    else dmao<((p) & 3) * 1024, S>(vx[(p) & 3], rX, sx0 + ks_ * 128); } while (0)
#define SDMA8(S) do { SM0(S, 0); asm volatile("s_nop 0"); SDMA(S, 0); SDMA(S, 1); SDMA(S, 2); SDMA(S, 3); \
    SM0(S, 1); asm volatile("s_nop 0"); SDMA(S, 4); SDMA(S, 5); SDMA(S, 6); SDMA(S, 7); } while (0)
  u32x2 fl; u32 dv;
  u32x4 gx[2], gwv; float gval; f32x2 gp0, gp1;
#define DPPA(c) gval += __int_as_float(__builtin_amdgcn_update_dpp(0, __float_as_int(gval), c, 0xf, 0xf, false))
  const rsrc_t rKC = mk(ssk + ch0 * 2048, 4096), rKC1 = mk(ssk + (ch0 + 1) * 2048, 4096);
  u32x4 kc[2][2];
  const u32 rtg = 4 * xcd + (jb & 3);
  const u32 gsl = g1ok ? rp * 16 + ct : (rtg >> 1) * 16 + 14 + (rtg & 1);
  SDMA8(0);

  waitv<10>();
  TIE1(pfd); TIE1(G); TIE4(kr[0], kr[1], kr[2], kr[3]); TIE1(kr[4]); TIE1(vr[0]); TIE1(vr[1]); TIE4(sk[0], sk[1], sk[2], sk[3]);
  STAMP(1);
  u32 e0, e1;
  { const u64 E = (((u64)G.y << 32) | G.x) + 1ull;
    e0 = __builtin_amdgcn_readfirstlane((u32)E); e1 = __builtin_amdgcn_readfirstlane((u32)(E >> 32)); }
  {
    LAS char* pk_ = lds + L_KV + c8 * 1024 + ((((31 - rr) >> 2) ^ c8) * 16) + ((31 - rr) & 3) * 4;
    f32x2 a0, a1, a2, a3, k0, k1, k2, k3, k4, p0, p1, p2, p3, s0;
#define PP0(m) a0 = f32x2{blo(sk[m][0]), blo(sk[m][2])}; a1 = f32x2{bhi(sk[m][0]), bhi(sk[m][2])}; \
      a2 = f32x2{blo(sk[m][1]), blo(sk[m][3])}; a3 = f32x2{bhi(sk[m][1]), bhi(sk[m][3])}; k0 = up(kr[0][m]); k1 = up(kr[1][m]); \
      TIE4(a0, a1, a2, a3); TIE1(k0); TIE1(k1)
#define PP1(m) k2 = up(kr[2][m]); k3 = up(kr[3][m]); k4 = up(kr[4][m]); p0 = rnd(k0 * a3); p1 = rnd(k1 * a2); TIE4(k2, k3, k4, p0); TIE1(p1)
#define PP2(m) p2 = rnd(k2 * a1); p3 = rnd(k3 * a0); p0 = rnd(rnd(p0 + p1) + p2); TIE1(p0); TIE1(p3)
#define PP3(m) s0 = rnd(p0 + p3) * up(vr[0][m]); p0 = rnd(k1 * a3); p1 = rnd(k2 * a2); TIE1(s0); TIE1(p0); TIE1(p1)
#define PP4(m) p2 = rnd(k3 * a1); p3 = rnd(k4 * a0); p0 = rnd(rnd(p0 + p1) + p2); TIE1(p0); TIE1(p3)
#define PP5(m) p0 = rnd(p0 + p3) * up(vr[1][m]); \
      *(LAS u32*)(pk_ + (2 * m) * 128) = pk(p0[0], s0[0]); *(LAS u32*)(pk_ + (2 * m + 1) * 128) = pk(p0[1], s0[1])
     SB(); PP0(0); SB();
     SB(); PP1(0); SB();
     SB(); PP2(0); SB();
     SB(); PP3(0); SB();
     SB(); PP4(0); SB();
     SB(); PP5(0); SB();
     SB(); PP0(1); SB();
     SB(); PP1(1); SB();
     SB(); PP2(1); SB();
     SB(); PP3(1); SB();
     SB(); PP4(1); SB();
     SB(); PP5(1); SB();
     SB(); PP0(2); SB();
     SB(); PP1(2); SB();
     SB(); PP2(2); SB();
     SB(); PP3(2); SB();
    SM0(1, 0); SB(); PP4(2); SB(); SDMA(1, 0);
     SB(); PP5(2); SB(); SDMA(1, 1);
     SB(); PP0(3); SB(); SDMA(1, 2);
     SB(); PP1(3); SB(); SDMA(1, 3);
    SM0(1, 1); SB(); PP2(3); SB(); SDMA(1, 4);
     SB(); PP3(3); SB(); SDMA(1, 5);
     SB(); PP4(3); SB(); SDMA(1, 6);
     SB(); PP5(3); SB(); SDMA(1, 7);
  }
  STAMP(12);
  waitv_bar<16, 9>();
  {
    const u32 qq = r16 >> 2, pp = r16 & 3;
    UNR for (int it = 0; it < 2; ++it)
      st16c<0>(*(LAS u32x4*)(lds + L_KV + (16 * w + 8 * it + (l >> 3)) * 128 + (((l & 7) ^ ((2 * w + it) & 7)) * 16)), (l >> 3) * 4096 + (l & 7) * 16,
               rWS, WS_UT + (64 * cg + 16 * w + 8 * it) * 4096 + (1984 - 64 * rt) * 2);
    const rsrc_t rW = mk(ws, w2ok ? WS_END : 0);
    UNR for (int ph = 0; ph < 2; ++ph)
      st16c<0>(trfrag(lds + L_W2S + ph * 4096 + (4 * g + qq) * 128 + (16 * w + 4 * pp) * 2, 2048), l * 16, rW,
               WS_W2F + ((cg * 56 + 2 * rt + ph) * 4 + w) * 1024);
  }
  STAMP(13);

  STAMP(2);

  f32x4 acc1[4][4];
  u32x4 fa[2][4], fb[2][4];
  LAS char* pa[2][4]; LAS char* pb[2][2];
  UNR for (int i = 0; i < 2; ++i) {
    const u32 rho = 4 * g + (r16 >> 2);
    UNR for (int ft = 0; ft < 4; ++ft)
      pa[i][ft] = opq(lds + L_RING + 2 * i * SLOT + 16384 + rho * 256 + (((4 * (w >> 1) + ft) ^ (rho & 7)) * 32) + (r16 & 3) * 8);
    const u32 o = L_RING + 2 * i * SLOT + (64 * (w & 1) + r16) * 128 + ((g ^ (r16 >> 1)) * 16);
    pb[i][0] = opq(lds + o); pb[i][1] = opq(lds + (o ^ 64));
  }
#define RA(J, ft) fa[(J) & 1][ft] = trfrag(pa[((J) >> 2) & 1][ft] + (((J) >> 1) & 1) * SLOT + ((J) & 1) * 8192, 4096)
#define RB(J, st) fb[(J) & 1][st] = *(LAS u32x4*)(pb[((J) >> 2) & 1][(J) & 1] + (((J) >> 1) & 1) * SLOT + (st) * 2048)
  u32x4 ur[2], wa[14][4];
  u32x4 cA[2][8], cB[2][8];
  const u32 chw = w >> 1, hw = w & 1;
  LAS char* pA; LAS char* pB;
  {
    const u32 x0 = r16 + 1, c = x0 & 7;
    pA = opq(lds + L_KC + chw * KC_CH + (272 * c + ((0xC0B6E590u >> (4 * c)) & 15) + (x0 >> 3) + g + 16 * hw) * 16);
    pB = opq(lds + L_R + chw * R_CH + 4064 - 32 * r16 + 16 * g + 256 * hw - 3584);
  }
#define RCA(r, i) cA[(r) & 1][i] = *(LAS u32x4*)(pA + (8 * (i) + (r)) * 64)
#define RCB(r, i) cB[(r) & 1][i] = *(LAS u32x4*)(pB + 3584 - 512 * (i) + 64 * (r))
  u32 ph[14];
  UNR for (int i = 0; i < 14; ++i) asm volatile("s_mov_b32 %0, %1" : "=s"(ph[i]) : "n"(i * 4096));
  const rsrc_t rWF = mk(ws + WS_W2F + (cg * 56 + 14 * w) * 4096, 14 * 4096), rHF = mk(ws + WS_HF + (rt * 56 + 14 * w) * 4096, 14 * 4096);
#define WAL(n) ld16ca<((n) & 3) * 1024>(wa[(n) >> 2][(n) & 3], l * 16, rWF, ph[(n) >> 2])
#define KCW(CH, c) do { const u32x4 A_ = kc[CH][0], B_ = kc[CH][1]; const u32 d_[8] = {A_.x, A_.y, A_.z, A_.w, B_.x, B_.y, B_.z, B_.w}; \
    LAS char* pk_ = lds + L_KC + (CH) * KC_CH + (tid + 1) * 16; constexpr int hb_ = (c) >> 1; \
    if ((c) == 0) *(LAS u32x4*)(pk_ + 16) = B_; \
    else *(LAS u32x4*)(pk_ + (272 * (c) + ((0xC0B6E590u >> (4 * (c))) & 15)) * 16) = ((c) & 1) ? \
      u32x4{__builtin_amdgcn_alignbyte(d_[hb_ + 1], d_[hb_], 2), __builtin_amdgcn_alignbyte(d_[hb_ + 2], d_[hb_ + 1], 2), \
            __builtin_amdgcn_alignbyte(d_[hb_ + 3], d_[hb_ + 2], 2), __builtin_amdgcn_alignbyte(d_[(hb_ + 4) & 7], d_[hb_ + 3], 2)} : \
      u32x4{d_[hb_], d_[hb_ + 1], d_[hb_ + 2], d_[hb_ + 3]}; } while (0)

  waitv_bar<12, 0>();
  RB(0, 0); RA(0, 0); RB(0, 1); RB(0, 2); RB(0, 3); RA(0, 1); RA(0, 2); RA(0, 3);
  STAMP(3);
  const u32 vkz = tid < 16 ? (L_KC + (tid >> 3) * KC_CH + (272 * (tid & 7) + ((0xC0B6E590u >> (4 * (tid & 7))) & 15) + ((tid & 7) == 0)) * 16)
                           : (tid < 76 ? L_R + (tid >= 46 ? R_CH - 480 : 0) + 4096 + (tid - 16) * 16 : L_R + 9216);
  SM0(2, 0); mf<true>(acc1[0][0], fa[0][0], fb[0][0]); RB(1, 0); SB(); mf<true>(acc1[0][1], fa[0][0], fb[0][1]); RA(1, 0); SDMA(2, 0); SB();
  mf<true>(acc1[0][2], fa[0][0], fb[0][2]); RB(1, 1); SB(); mf<true>(acc1[0][3], fa[0][0], fb[0][3]); RB(1, 2); SDMA(2, 1); SB();
  mf<true>(acc1[1][0], fa[0][1], fb[0][0]); RB(1, 3); SB(); mf<true>(acc1[1][1], fa[0][1], fb[0][1]); RA(1, 1); SDMA(2, 2); SB();
  mf<true>(acc1[1][2], fa[0][1], fb[0][2]); SB(); mf<true>(acc1[1][3], fa[0][1], fb[0][3]); RA(1, 2); SDMA(2, 3); SB();
  mf<true>(acc1[2][0], fa[0][2], fb[0][0]); SB(); mf<true>(acc1[2][1], fa[0][2], fb[0][1]); RA(1, 3); SM0(2, 1); asm volatile("s_nop 0"); SDMA(2, 4);
  SB(); mf<true>(acc1[2][2], fa[0][2], fb[0][2]); SB(); mf<true>(acc1[2][3], fa[0][2], fb[0][3]); SDMA(2, 5); SB();
  mf<true>(acc1[3][0], fa[0][3], fb[0][0]); SB(); mf<true>(acc1[3][1], fa[0][3], fb[0][1]); SDMA(2, 6); SB();
  mf<true>(acc1[3][2], fa[0][3], fb[0][2]); SB(); mf<true>(acc1[3][3], fa[0][3], fb[0][3]); SDMA(2, 7); SB();
  waitv_bar<12, 1>(); SM0(3, 0); mf<false>(acc1[0][0], fa[1][0], fb[1][0]); RB(2, 0); SB(); mf<false>(acc1[0][1], fa[1][0], fb[1][1]); RA(2, 0);
  SDMA(3, 0); SB(); mf<false>(acc1[0][2], fa[1][0], fb[1][2]); RB(2, 1); SB(); mf<false>(acc1[0][3], fa[1][0], fb[1][3]); RB(2, 2); SDMA(3, 1); SB();
  mf<false>(acc1[1][0], fa[1][1], fb[1][0]); RB(2, 3); SB(); mf<false>(acc1[1][1], fa[1][1], fb[1][1]); RA(2, 1); SDMA(3, 2); SB();
  mf<false>(acc1[1][2], fa[1][1], fb[1][2]); SB(); mf<false>(acc1[1][3], fa[1][1], fb[1][3]); RA(2, 2); SDMA(3, 3); SB();
  mf<false>(acc1[2][0], fa[1][2], fb[1][0]); SB(); mf<false>(acc1[2][1], fa[1][2], fb[1][1]); RA(2, 3);
  SM0(3, 1); asm volatile("s_nop 0"); SDMA(3, 4); SB(); mf<false>(acc1[2][2], fa[1][2], fb[1][2]); SB(); mf<false>(acc1[2][3], fa[1][2], fb[1][3]);
  SDMA(3, 5); SB(); mf<false>(acc1[3][0], fa[1][3], fb[1][0]); SB(); mf<false>(acc1[3][1], fa[1][3], fb[1][1]); SDMA(3, 6); SB();
  mf<false>(acc1[3][2], fa[1][3], fb[1][2]); SB(); mf<false>(acc1[3][3], fa[1][3], fb[1][3]); SDMA(3, 7); SB();
  SM0(4, 0); mf<false>(acc1[0][0], fa[0][0], fb[0][0]); RB(3, 0); SB(); mf<false>(acc1[0][1], fa[0][0], fb[0][1]); RA(3, 0); SDMA(4, 0); SB();
  mf<false>(acc1[0][2], fa[0][0], fb[0][2]); RB(3, 1); SB(); mf<false>(acc1[0][3], fa[0][0], fb[0][3]); RB(3, 2); SB();
  mf<false>(acc1[1][0], fa[0][1], fb[0][0]); RB(3, 3); SB(); mf<false>(acc1[1][1], fa[0][1], fb[0][1]); RA(3, 1); SDMA(4, 1); SB();
  mf<false>(acc1[1][2], fa[0][1], fb[0][2]); SB(); mf<false>(acc1[1][3], fa[0][1], fb[0][3]); RA(3, 2); SB();
  mf<false>(acc1[2][0], fa[0][2], fb[0][0]); SB(); mf<false>(acc1[2][1], fa[0][2], fb[0][1]); RA(3, 3); SDMA(4, 2); SB();
  mf<false>(acc1[2][2], fa[0][2], fb[0][2]); SB(); mf<false>(acc1[2][3], fa[0][2], fb[0][3]); SB(); mf<false>(acc1[3][0], fa[0][3], fb[0][0]); SB();
  mf<false>(acc1[3][1], fa[0][3], fb[0][1]); SDMA(4, 3); SB(); mf<false>(acc1[3][2], fa[0][3], fb[0][2]); SB();
  mf<false>(acc1[3][3], fa[0][3], fb[0][3]); SB();
  waitv_bar<12, 2>(); SM0(4, 1); mf<false>(acc1[0][0], fa[1][0], fb[1][0]); RB(4, 0); SB(); mf<false>(acc1[0][1], fa[1][0], fb[1][1]); RA(4, 0);
  SDMA(4, 4); SB(); mf<false>(acc1[0][2], fa[1][0], fb[1][2]); RB(4, 1); SB(); mf<false>(acc1[0][3], fa[1][0], fb[1][3]); RB(4, 2); SB();
  mf<false>(acc1[1][0], fa[1][1], fb[1][0]); RB(4, 3); SB(); mf<false>(acc1[1][1], fa[1][1], fb[1][1]); RA(4, 1); SDMA(4, 5); SB();
  mf<false>(acc1[1][2], fa[1][1], fb[1][2]); SB(); mf<false>(acc1[1][3], fa[1][1], fb[1][3]); RA(4, 2); SB();
  mf<false>(acc1[2][0], fa[1][2], fb[1][0]); SB(); mf<false>(acc1[2][1], fa[1][2], fb[1][1]); RA(4, 3); SDMA(4, 6); SB();
  mf<false>(acc1[2][2], fa[1][2], fb[1][2]); SB(); mf<false>(acc1[2][3], fa[1][2], fb[1][3]); SB(); mf<false>(acc1[3][0], fa[1][3], fb[1][0]); SB();
  mf<false>(acc1[3][1], fa[1][3], fb[1][1]); SDMA(4, 7); SB(); mf<false>(acc1[3][2], fa[1][3], fb[1][2]); SB();
  mf<false>(acc1[3][3], fa[1][3], fb[1][3]); SB();
  SM0(5, 0); mf<false>(acc1[0][0], fa[0][0], fb[0][0]); RB(5, 0); SB(); mf<false>(acc1[0][1], fa[0][0], fb[0][1]); RA(5, 0); SDMA(5, 0); SB();
  mf<false>(acc1[0][2], fa[0][0], fb[0][2]); RB(5, 1); SB(); mf<false>(acc1[0][3], fa[0][0], fb[0][3]); RB(5, 2); SDMA(5, 1); SB();
  mf<false>(acc1[1][0], fa[0][1], fb[0][0]); RB(5, 3); SB(); mf<false>(acc1[1][1], fa[0][1], fb[0][1]); RA(5, 1); SB();
  mf<false>(acc1[1][2], fa[0][1], fb[0][2]); SDMA(5, 2); SB(); mf<false>(acc1[1][3], fa[0][1], fb[0][3]); RA(5, 2); SB();
  mf<false>(acc1[2][0], fa[0][2], fb[0][0]); SB(); mf<false>(acc1[2][1], fa[0][2], fb[0][1]); RA(5, 3); SDMA(5, 3); SB();
  mf<false>(acc1[2][2], fa[0][2], fb[0][2]); SB(); mf<false>(acc1[2][3], fa[0][2], fb[0][3]); ld16<0>(kc[0][0], tid * 16 - 16, rKC, Z); SB();
  mf<false>(acc1[3][0], fa[0][3], fb[0][0]); SB(); mf<false>(acc1[3][1], fa[0][3], fb[0][1]); SB(); mf<false>(acc1[3][2], fa[0][3], fb[0][2]);
  ld16<0>(kc[0][1], tid * 16, rKC, Z); SB(); mf<false>(acc1[3][3], fa[0][3], fb[0][3]); SB();
  waitv_bar<14, 3>(); st8c<0>(u32x2{e0, e1}, vone, rWS, WS_F1 + (cg * 32 + rt) * 8); SM0(5, 1); mf<false>(acc1[0][0], fa[1][0], fb[1][0]); RB(6, 0);
  SB(); mf<false>(acc1[0][1], fa[1][0], fb[1][1]); RA(6, 0); SDMA(5, 4); SB(); mf<false>(acc1[0][2], fa[1][0], fb[1][2]); RB(6, 1); SB();
  mf<false>(acc1[0][3], fa[1][0], fb[1][3]); RB(6, 2); SDMA(5, 5); SB(); mf<false>(acc1[1][0], fa[1][1], fb[1][0]); RB(6, 3); SB();
  mf<false>(acc1[1][1], fa[1][1], fb[1][1]); RA(6, 1); SB(); mf<false>(acc1[1][2], fa[1][1], fb[1][2]); SDMA(5, 6); SB();
  mf<false>(acc1[1][3], fa[1][1], fb[1][3]); RA(6, 2); SB(); mf<false>(acc1[2][0], fa[1][2], fb[1][0]); SB();
  mf<false>(acc1[2][1], fa[1][2], fb[1][1]); RA(6, 3); SDMA(5, 7); SB(); mf<false>(acc1[2][2], fa[1][2], fb[1][2]); SB();
  mf<false>(acc1[2][3], fa[1][2], fb[1][3]); ld16<0>(kc[1][0], tid * 16 - 16, rKC1, Z); SB(); mf<false>(acc1[3][0], fa[1][3], fb[1][0]); SB();
  mf<false>(acc1[3][1], fa[1][3], fb[1][1]); SB(); mf<false>(acc1[3][2], fa[1][3], fb[1][2]); ld16<0>(kc[1][1], tid * 16, rKC1, Z); SB();
  mf<false>(acc1[3][3], fa[1][3], fb[1][3]); SB();
  SM0(6, 0); mf<false>(acc1[0][0], fa[0][0], fb[0][0]); RB(7, 0); SB(); mf<false>(acc1[0][1], fa[0][0], fb[0][1]); RA(7, 0); SDMA(6, 0); SB();
  mf<false>(acc1[0][2], fa[0][0], fb[0][2]); RB(7, 1); SB(); mf<false>(acc1[0][3], fa[0][0], fb[0][3]); RB(7, 2); SB();
  mf<false>(acc1[1][0], fa[0][1], fb[0][0]); RB(7, 3); SDMA(6, 1); SB(); mf<false>(acc1[1][1], fa[0][1], fb[0][1]); RA(7, 1); SB();
  mf<false>(acc1[1][2], fa[0][1], fb[0][2]); SB(); mf<false>(acc1[1][3], fa[0][1], fb[0][3]); RA(7, 2); SDMA(6, 2); SB();
  mf<false>(acc1[2][0], fa[0][2], fb[0][0]); SB(); mf<false>(acc1[2][1], fa[0][2], fb[0][1]); RA(7, 3); SB();
  mf<false>(acc1[2][2], fa[0][2], fb[0][2]); SDMA(6, 3); SB(); mf<false>(acc1[2][3], fa[0][2], fb[0][3]); SB();
  mf<false>(acc1[3][0], fa[0][3], fb[0][0]); SB(); mf<false>(acc1[3][1], fa[0][3], fb[0][1]); ld2(dv, 0, mk(ssd, 1024), (ch0 + (w >> 1)) * 2); SB();
  mf<false>(acc1[3][2], fa[0][3], fb[0][2]); SB(); mf<false>(acc1[3][3], fa[0][3], fb[0][3]); SB();
  waitv_bar<18, 4>(); SM0(6, 1); mf<false>(acc1[0][0], fa[1][0], fb[1][0]); RB(8, 0); SB(); mf<false>(acc1[0][1], fa[1][0], fb[1][1]); RA(8, 0);
  SDMA(6, 4); SB(); mf<false>(acc1[0][2], fa[1][0], fb[1][2]); RB(8, 1); SB(); mf<false>(acc1[0][3], fa[1][0], fb[1][3]); RB(8, 2); SB();
  mf<false>(acc1[1][0], fa[1][1], fb[1][0]); RB(8, 3); SB(); mf<false>(acc1[1][1], fa[1][1], fb[1][1]); RA(8, 1); SDMA(6, 5); SB();
  mf<false>(acc1[1][2], fa[1][1], fb[1][2]); SB(); mf<false>(acc1[1][3], fa[1][1], fb[1][3]); RA(8, 2); SB();
  mf<false>(acc1[2][0], fa[1][2], fb[1][0]); SB(); mf<false>(acc1[2][1], fa[1][2], fb[1][1]); RA(8, 3); SDMA(6, 6); SB();
  mf<false>(acc1[2][2], fa[1][2], fb[1][2]); SB(); mf<false>(acc1[2][3], fa[1][2], fb[1][3]); SB(); mf<false>(acc1[3][0], fa[1][3], fb[1][0]); SB();
  mf<false>(acc1[3][1], fa[1][3], fb[1][1]); SDMA(6, 7); SB(); mf<false>(acc1[3][2], fa[1][3], fb[1][2]); SB();
  mf<false>(acc1[3][3], fa[1][3], fb[1][3]); SB();
  SM0(7, 0); mf<false>(acc1[0][0], fa[0][0], fb[0][0]); RB(9, 0); SB(); mf<false>(acc1[0][1], fa[0][0], fb[0][1]); RA(9, 0); SDMA(7, 0); SB();
  mf<false>(acc1[0][2], fa[0][0], fb[0][2]); RB(9, 1); SB(); mf<false>(acc1[0][3], fa[0][0], fb[0][3]); RB(9, 2); SB();
  mf<false>(acc1[1][0], fa[0][1], fb[0][0]); RB(9, 3); SB(); mf<false>(acc1[1][1], fa[0][1], fb[0][1]); RA(9, 1); SDMA(7, 1); SB();
  mf<false>(acc1[1][2], fa[0][1], fb[0][2]); SB(); mf<false>(acc1[1][3], fa[0][1], fb[0][3]); RA(9, 2); SB();
  mf<false>(acc1[2][0], fa[0][2], fb[0][0]); SB(); mf<false>(acc1[2][1], fa[0][2], fb[0][1]); RA(9, 3); SDMA(7, 2); SB();
  mf<false>(acc1[2][2], fa[0][2], fb[0][2]); SB(); mf<false>(acc1[2][3], fa[0][2], fb[0][3]); SB(); mf<false>(acc1[3][0], fa[0][3], fb[0][0]); SB();
  mf<false>(acc1[3][1], fa[0][3], fb[0][1]); SDMA(7, 3); SB(); mf<false>(acc1[3][2], fa[0][3], fb[0][2]); SB();
  mf<false>(acc1[3][3], fa[0][3], fb[0][3]); SB();
  waitv_bar<15, 5>(); SM0(7, 1); mf<false>(acc1[0][0], fa[1][0], fb[1][0]); RB(10, 0); SB(); mf<false>(acc1[0][1], fa[1][0], fb[1][1]); RA(10, 0);
  SDMA(7, 4); SB(); mf<false>(acc1[0][2], fa[1][0], fb[1][2]); RB(10, 1); SB(); mf<false>(acc1[0][3], fa[1][0], fb[1][3]); RB(10, 2); SB();
  mf<false>(acc1[1][0], fa[1][1], fb[1][0]); RB(10, 3); SB(); mf<false>(acc1[1][1], fa[1][1], fb[1][1]); RA(10, 1); SDMA(7, 5); SB();
  mf<false>(acc1[1][2], fa[1][1], fb[1][2]); SB(); mf<false>(acc1[1][3], fa[1][1], fb[1][3]); RA(10, 2); SB();
  mf<false>(acc1[2][0], fa[1][2], fb[1][0]); SB(); mf<false>(acc1[2][1], fa[1][2], fb[1][1]); RA(10, 3); SDMA(7, 6); SB();
  mf<false>(acc1[2][2], fa[1][2], fb[1][2]); SB(); mf<false>(acc1[2][3], fa[1][2], fb[1][3]); SB(); mf<false>(acc1[3][0], fa[1][3], fb[1][0]); SB();
  mf<false>(acc1[3][1], fa[1][3], fb[1][1]); SDMA(7, 7); SB(); mf<false>(acc1[3][2], fa[1][3], fb[1][2]); SB();
  mf<false>(acc1[3][3], fa[1][3], fb[1][3]); SB();
  mf<false>(acc1[0][0], fa[0][0], fb[0][0]); RB(11, 0); SB(); mf<false>(acc1[0][1], fa[0][0], fb[0][1]); RA(11, 0);
  ld16<0>(gx[0], l * 16, rQ, (8 * gsl + 2 * w) * 1024); SB(); mf<false>(acc1[0][2], fa[0][0], fb[0][2]); RB(11, 1); SB();
  mf<false>(acc1[0][3], fa[0][0], fb[0][3]); RB(11, 2); SB(); mf<false>(acc1[1][0], fa[0][1], fb[0][0]); RB(11, 3); SB();
  mf<false>(acc1[1][1], fa[0][1], fb[0][1]); RA(11, 1); ld16<1024>(gx[1], l * 16, rQ, (8 * gsl + 2 * w) * 1024); SB();
  mf<false>(acc1[1][2], fa[0][1], fb[0][2]); SB(); mf<false>(acc1[1][3], fa[0][1], fb[0][3]); RA(11, 2); SB();
  mf<false>(acc1[2][0], fa[0][2], fb[0][0]); SB(); mf<false>(acc1[2][1], fa[0][2], fb[0][1]); RA(11, 3); ld16<0>(gwv, l * 16, mk(gw, 1024), Z); SB();
  mf<false>(acc1[2][2], fa[0][2], fb[0][2]); SB(); mf<false>(acc1[2][3], fa[0][2], fb[0][3]); SB(); mf<false>(acc1[3][0], fa[0][3], fb[0][0]); SB();
  mf<false>(acc1[3][1], fa[0][3], fb[0][1]); ld8c(fl, (l & 31) * 8, rWS, WS_F1 + cg * 256); SB(); mf<false>(acc1[3][2], fa[0][3], fb[0][2]); SB();
  mf<false>(acc1[3][3], fa[0][3], fb[0][3]); SB();
  waitv_bar<12, 6>(); TIE4(kc[0][0], kc[0][1], kc[1][0], kc[1][1]); TIE1(dv); mf<false>(acc1[0][0], fa[1][0], fb[1][0]); RB(12, 0); SB();
  mf<false>(acc1[0][1], fa[1][0], fb[1][1]); RA(12, 0); SB(); mf<false>(acc1[0][2], fa[1][0], fb[1][2]); RB(12, 1); SB();
  mf<false>(acc1[0][3], fa[1][0], fb[1][3]); RB(12, 2); SB(); mf<false>(acc1[1][0], fa[1][1], fb[1][0]); RB(12, 3); SB();
  mf<false>(acc1[1][1], fa[1][1], fb[1][1]); RA(12, 1); SB(); mf<false>(acc1[1][2], fa[1][1], fb[1][2]); KCW(0, 0); SB();
  mf<false>(acc1[1][3], fa[1][1], fb[1][3]); RA(12, 2); SB(); mf<false>(acc1[2][0], fa[1][2], fb[1][0]); KCW(0, 1); SB();
  mf<false>(acc1[2][1], fa[1][2], fb[1][1]); RA(12, 3); SB(); mf<false>(acc1[2][2], fa[1][2], fb[1][2]); SB();
  mf<false>(acc1[2][3], fa[1][2], fb[1][3]); KCW(0, 2); SB(); mf<false>(acc1[3][0], fa[1][3], fb[1][0]); SB();
  mf<false>(acc1[3][1], fa[1][3], fb[1][1]); KCW(0, 3); SB(); mf<false>(acc1[3][2], fa[1][3], fb[1][2]); SB();
  mf<false>(acc1[3][3], fa[1][3], fb[1][3]); SB();
  mf<false>(acc1[0][0], fa[0][0], fb[0][0]); RB(13, 0); SB(); mf<false>(acc1[0][1], fa[0][0], fb[0][1]); RA(13, 0); SB();
  mf<false>(acc1[0][2], fa[0][0], fb[0][2]); RB(13, 1); SB(); mf<false>(acc1[0][3], fa[0][0], fb[0][3]); RB(13, 2); SB();
  mf<false>(acc1[1][0], fa[0][1], fb[0][0]); RB(13, 3); SB(); mf<false>(acc1[1][1], fa[0][1], fb[0][1]); RA(13, 1); SB();
  mf<false>(acc1[1][2], fa[0][1], fb[0][2]); KCW(0, 4); SB(); mf<false>(acc1[1][3], fa[0][1], fb[0][3]); RA(13, 2); SB();
  mf<false>(acc1[2][0], fa[0][2], fb[0][0]); KCW(0, 5); SB(); mf<false>(acc1[2][1], fa[0][2], fb[0][1]); RA(13, 3); SB();
  mf<false>(acc1[2][2], fa[0][2], fb[0][2]); SB(); mf<false>(acc1[2][3], fa[0][2], fb[0][3]); KCW(0, 6); SB();
  mf<false>(acc1[3][0], fa[0][3], fb[0][0]); SB(); mf<false>(acc1[3][1], fa[0][3], fb[0][1]); KCW(0, 7); SB();
  mf<false>(acc1[3][2], fa[0][3], fb[0][2]); *(LAS u32x4*)(lds + vkz) = u32x4{0, 0, 0, 0}; SB(); mf<false>(acc1[3][3], fa[0][3], fb[0][3]); SB();
  waitv_bar<0, 7>(); TIE1(gx[0]); TIE1(gx[1]); TIE1(gwv); poll(fl, (l & 31) * 8, rWS, WS_F1 + cg * 256, e0, e1); STAMP(4);
  mf<false>(acc1[0][0], fa[1][0], fb[1][0]); RB(14, 0); SB(); mf<false>(acc1[0][1], fa[1][0], fb[1][1]); RA(14, 0);
  ld16c<0>(ur[0], tid * 16, rWS, WS_UT + ch0 * 4096); SB(); mf<false>(acc1[0][2], fa[1][0], fb[1][2]); RB(14, 1); SB();
  mf<false>(acc1[0][3], fa[1][0], fb[1][3]); RB(14, 2); ld16c<0>(ur[1], tid * 16, rWS, WS_UT + ch0 * 4096 + 4096); SB();
  mf<false>(acc1[1][0], fa[1][1], fb[1][0]); RB(14, 3); SB(); mf<false>(acc1[1][1], fa[1][1], fb[1][1]); RA(14, 1); SB();
  mf<false>(acc1[1][2], fa[1][1], fb[1][2]); WAL(0); KCW(1, 0); SB(); mf<false>(acc1[1][3], fa[1][1], fb[1][3]); RA(14, 2); SB();
  mf<false>(acc1[2][0], fa[1][2], fb[1][0]); KCW(1, 1); SB(); mf<false>(acc1[2][1], fa[1][2], fb[1][1]); RA(14, 3); WAL(1); SB();
  mf<false>(acc1[2][2], fa[1][2], fb[1][2]); SB(); mf<false>(acc1[2][3], fa[1][2], fb[1][3]); WAL(2); KCW(1, 2); SB();
  mf<false>(acc1[3][0], fa[1][3], fb[1][0]); SB(); mf<false>(acc1[3][1], fa[1][3], fb[1][1]); KCW(1, 3); SB();
  mf<false>(acc1[3][2], fa[1][3], fb[1][2]); WAL(3); SB(); mf<false>(acc1[3][3], fa[1][3], fb[1][3]); SB();
  mf<false>(acc1[0][0], fa[0][0], fb[0][0]); RB(15, 0); SB(); mf<false>(acc1[0][1], fa[0][0], fb[0][1]); RA(15, 0); WAL(4); SB();
  mf<false>(acc1[0][2], fa[0][0], fb[0][2]); RB(15, 1); SB(); mf<false>(acc1[0][3], fa[0][0], fb[0][3]); RB(15, 2); WAL(5); SB();
  mf<false>(acc1[1][0], fa[0][1], fb[0][0]); RB(15, 3); SB(); mf<false>(acc1[1][1], fa[0][1], fb[0][1]); RA(15, 1); SB();
  mf<false>(acc1[1][2], fa[0][1], fb[0][2]); WAL(6); KCW(1, 4); gp0 = f32x2{0.f, 0.f}; gp1 = gp0; SB(); mf<false>(acc1[1][3], fa[0][1], fb[0][3]);
  RA(15, 2); SB(); mf<false>(acc1[2][0], fa[0][2], fb[0][0]); KCW(1, 5);
  { const f32x2 gq = up(gwv[0]); gp0 += rnd(up(gx[0][0]) * gq); gp1 += rnd(up(gx[1][0]) * gq); TIE1(gp0); TIE1(gp1); } SB();
  mf<false>(acc1[2][1], fa[0][2], fb[0][1]); RA(15, 3); WAL(7); SB(); mf<false>(acc1[2][2], fa[0][2], fb[0][2]);
  { const f32x2 gq = up(gwv[1]); gp0 += rnd(up(gx[0][1]) * gq); gp1 += rnd(up(gx[1][1]) * gq); TIE1(gp0); TIE1(gp1); } SB();
  mf<false>(acc1[2][3], fa[0][2], fb[0][3]); WAL(8); KCW(1, 6); SB(); mf<false>(acc1[3][0], fa[0][3], fb[0][0]);
  { const f32x2 gq = up(gwv[2]); gp0 += rnd(up(gx[0][2]) * gq); gp1 += rnd(up(gx[1][2]) * gq); TIE1(gp0); TIE1(gp1); } SB();
  mf<false>(acc1[3][1], fa[0][3], fb[0][1]); KCW(1, 7); SB(); mf<false>(acc1[3][2], fa[0][3], fb[0][2]); WAL(9);
  { const f32x2 gq = up(gwv[3]); gp0 += rnd(up(gx[0][3]) * gq); gp1 += rnd(up(gx[1][3]) * gq); TIE1(gp0); TIE1(gp1); } SB();
  mf<false>(acc1[3][3], fa[0][3], fb[0][3]); SB();
  lbar(); mf<false>(acc1[0][0], fa[1][0], fb[1][0]); RCA(0, 0); SB(); mf<false>(acc1[0][1], fa[1][0], fb[1][1]); WAL(10);
  { auto s = __builtin_amdgcn_permlane32_swap(__float_as_uint(gp0[0] + gp0[1]), __float_as_uint(gp1[0] + gp1[1]), false, false); gval = __uint_as_float(s[0]) + __uint_as_float(s[1]); TIE1(gval); }
  SB(); mf<false>(acc1[0][2], fa[1][0], fb[1][2]); RCA(0, 1); SB(); mf<false>(acc1[0][3], fa[1][0], fb[1][3]); WAL(11);
  { auto s2 = __builtin_amdgcn_permlane16_swap(__float_as_uint(gval), __float_as_uint(gval), false, false); gval = __uint_as_float(s2[0]) + __uint_as_float(s2[1]); DPPA(0x128); TIE1(gval); }
  SB(); mf<false>(acc1[1][0], fa[1][1], fb[1][0]); RCA(0, 2); SB(); mf<false>(acc1[1][1], fa[1][1], fb[1][1]); DPPA(0x124); DPPA(0x122); TIE1(gval);
  SB(); mf<false>(acc1[1][2], fa[1][1], fb[1][2]); RCA(0, 3); WAL(12); SB(); mf<false>(acc1[1][3], fa[1][1], fb[1][3]);
  DPPA(0x121); gval = -blo(pk(gval, gval)); gval = 1.f + __expf(gval); TIE1(gval); SB(); mf<false>(acc1[2][0], fa[1][2], fb[1][0]); RCA(0, 4); SB();
  mf<false>(acc1[2][1], fa[1][2], fb[1][1]); WAL(13); gval = blo(pk(1.f / gval, 0.f)); TIE1(gval); SB(); mf<false>(acc1[2][2], fa[1][2], fb[1][2]);
  RCA(0, 5); SB(); mf<false>(acc1[2][3], fa[1][2], fb[1][3]); WAL(14); SB(); mf<false>(acc1[3][0], fa[1][3], fb[1][0]); RCA(0, 6);
  st4c(__float_as_uint(gval), (l & 31) ? 0x7fffff00u : (l >> 5) * 4, rWS, WS_GATE + (8 * gsl + 2 * w) * 4); SB();
  mf<false>(acc1[3][1], fa[1][3], fb[1][1]); SB(); mf<false>(acc1[3][2], fa[1][3], fb[1][2]); RCA(0, 7); WAL(15); SB();
  mf<false>(acc1[3][3], fa[1][3], fb[1][3]); SB();
  mfdone();
  STAMP(5);

  {
    const rsrc_t rH = mk(ws + WS_HF + ((2 * rp + (w & 1)) * 56 + 4 * ct + 2 * (w >> 1)) * 4096, g1ok ? 8192 : 0);
#define HST(u, st) do { const f32x4 a = acc1[2 * (u)][st], b = acc1[2 * (u) + 1][st]; \
      st16c<(st) * 1024>(u32x4{relu2(pk(a[0], a[1])), relu2(pk(a[2], a[3])), relu2(pk(b[0], b[1])), relu2(pk(b[2], b[3]))}, l * 16 + (u) * 4096, rH, Z); } while (0)
    HST(0, 0); HST(0, 1); HST(0, 2); HST(0, 3); HST(1, 0); HST(1, 1); HST(1, 2); HST(1, 3);
  }
  SB();
  WAL(16); WAL(17); waitv<27>(); TIE1(ur[0]); TIE1(ur[1]); *(LAS u32x4*)(lds + L_R + tid * 16) = ur[0];
  *(LAS u32x4*)(lds + L_R + R_CH + tid * 16) = ur[1]; lbar(); RCB(0, 0); RCB(0, 1); RCB(0, 2); RCB(0, 3); RCB(0, 4); RCB(0, 5); RCB(0, 6); RCB(0, 7);
  STAMP(6);

  f32x4 cacc[8];
  u32x4 hb[4][4], yt[2];
  u32x2 qv[4]; u32 gg;
  u32 vh[4];
  UNR for (int b = 0; b < 4; ++b) vh[b] = l * 16 + ((b + w) & 3) * 1024;
#define HBL(n) ld16c<0>(hb[((n) >> 2) % 4][(n) & 3], vh[(n) & 3], rHF, ph[(n) >> 2])
  const u32 sfh = WS_FH + gsl * 8;
#define HBEA() asm volatile("s_cmp_lg_u64 %15, 0\n\ts_cbranch_scc1 .Lhbe%=\n\tbuffer_load_dwordx4 %0, %8, %12, %13 offen sc1 ; PATHA\n\tbuffer_load_dwordx4 %1, %9, %12, %13 offen sc1 ; PATHA\n\tbuffer_load_dwordx4 %2, %10, %12, %13 offen sc1 ; PATHA\n\tbuffer_load_dwordx4 %3, %11, %12, %13 offen sc1 ; PATHA\n\tbuffer_load_dwordx4 %4, %8, %12, %14 offen sc1 ; PATHA\n\tbuffer_load_dwordx4 %5, %9, %12, %14 offen sc1 ; PATHA\n\tbuffer_load_dwordx4 %6, %10, %12, %14 offen sc1 ; PATHA\n\tbuffer_load_dwordx4 %7, %11, %12, %14 offen sc1 ; PATHA\n\t\n.Lhbe%=:" : "=&v"(hb[0][0]), "=&v"(hb[0][1]), "=&v"(hb[0][2]), "=&v"(hb[0][3]), "=&v"(hb[1][0]), "=&v"(hb[1][1]), "=&v"(hb[1][2]), "=&v"(hb[1][3]) \
    : "v"(vh[0]), "v"(vh[1]), "v"(vh[2]), "v"(vh[3]), "s"(rHF), "s"(ph[0]), "s"(ph[1]), "s"(nrdy) : "scc")
#define HBEB() asm volatile("s_cmp_eq_u64 %15, 0\n\ts_cbranch_scc1 .Lhbe%=\n\tbuffer_load_dwordx4 %0, %8, %12, %13 offen sc1 ; PATHB\n\tbuffer_load_dwordx4 %1, %9, %12, %13 offen sc1 ; PATHB\n\tbuffer_load_dwordx4 %2, %10, %12, %13 offen sc1 ; PATHB\n\tbuffer_load_dwordx4 %3, %11, %12, %13 offen sc1 ; PATHB\n\tbuffer_load_dwordx4 %4, %8, %12, %14 offen sc1 ; PATHB\n\tbuffer_load_dwordx4 %5, %9, %12, %14 offen sc1 ; PATHB\n\tbuffer_load_dwordx4 %6, %10, %12, %14 offen sc1 ; PATHB\n\tbuffer_load_dwordx4 %7, %11, %12, %14 offen sc1 ; PATHB\n\t\n.Lhbe%=:" : "+v"(hb[0][0]), "+v"(hb[0][1]), "+v"(hb[0][2]), "+v"(hb[0][3]), "+v"(hb[1][0]), "+v"(hb[1][1]), "+v"(hb[1][2]), "+v"(hb[1][3]) \
    : "v"(vh[0]), "v"(vh[1]), "v"(vh[2]), "v"(vh[3]), "s"(rHF), "s"(ph[0]), "s"(ph[1]), "s"(nrdy) : "scc")
  u64 nrdy; u32x2 fl2;
  LAS char* pw = opq(lds + L_RED + w * 8192 + ((4 * r16 + g) ^ ((r16 >> 1) & 3)) * 16);
#define CPW(i) *(LAS f32x4*)(pw + (i) * 1024) = cacc[i]
  mf<true>(cacc[0], cA[0][0], cB[0][0]); SB(); mf<true>(cacc[1], cA[0][0], cB[0][1]); WAL(18); SB(); mf<true>(cacc[2], cA[0][0], cB[0][2]); RCA(1, 0);
  SB(); mf<true>(cacc[3], cA[0][0], cB[0][3]); SB(); mf<true>(cacc[4], cA[0][0], cB[0][4]); RCB(1, 0); SB(); mf<true>(cacc[5], cA[0][0], cB[0][5]);
  WAL(19); SB(); mf<true>(cacc[6], cA[0][0], cB[0][6]); RCB(1, 1); SB(); mf<true>(cacc[7], cA[0][0], cB[0][7]); SB();
  mf<false>(cacc[1], cA[0][1], cB[0][0]); RCB(1, 2); SB(); mf<false>(cacc[2], cA[0][1], cB[0][1]); SB(); mf<false>(cacc[3], cA[0][1], cB[0][2]);
  RCB(1, 3); WAL(20); SB(); mf<false>(cacc[4], cA[0][1], cB[0][3]); SB(); mf<false>(cacc[5], cA[0][1], cB[0][4]); RCB(1, 4); SB();
  mf<false>(cacc[6], cA[0][1], cB[0][5]); SB(); mf<false>(cacc[7], cA[0][1], cB[0][6]); RCB(1, 5); WAL(21); SB();
  mf<false>(cacc[2], cA[0][2], cB[0][0]); SB(); mf<false>(cacc[3], cA[0][2], cB[0][1]); RCB(1, 6); SB(); mf<false>(cacc[4], cA[0][2], cB[0][2]); SB();
  mf<false>(cacc[5], cA[0][2], cB[0][3]); RCB(1, 7); SB(); mf<false>(cacc[6], cA[0][2], cB[0][4]); WAL(22); SB();
  mf<false>(cacc[7], cA[0][2], cB[0][5]); RCA(1, 1); SB(); mf<false>(cacc[3], cA[0][3], cB[0][0]); SB(); mf<false>(cacc[4], cA[0][3], cB[0][1]);
  RCA(1, 2); SB(); mf<false>(cacc[5], cA[0][3], cB[0][2]); WAL(23); SB(); mf<false>(cacc[6], cA[0][3], cB[0][3]); RCA(1, 3); SB();
  mf<false>(cacc[7], cA[0][3], cB[0][4]); SB(); mf<false>(cacc[4], cA[0][4], cB[0][0]); RCA(1, 4); SB(); mf<false>(cacc[5], cA[0][4], cB[0][1]); SB();
  mf<false>(cacc[6], cA[0][4], cB[0][2]); RCA(1, 5); WAL(24); SB(); mf<false>(cacc[7], cA[0][4], cB[0][3]); SB();
  mf<false>(cacc[5], cA[0][5], cB[0][0]); RCA(1, 6); SB(); mf<false>(cacc[6], cA[0][5], cB[0][1]); SB(); mf<false>(cacc[7], cA[0][5], cB[0][2]);
  RCA(1, 7); WAL(25); SB(); mf<false>(cacc[6], cA[0][6], cB[0][0]); SB(); mf<false>(cacc[7], cA[0][6], cB[0][1]); SB();
  mf<false>(cacc[7], cA[0][7], cB[0][0]); SB();
  waitv_bar<10, 99>(); st8c<0>(u32x2{e0, e1}, vone, rWS, sfh); STAMP(7); mf<false>(cacc[0], cA[1][0], cB[1][0]); SB();
  mf<false>(cacc[1], cA[1][0], cB[1][1]); WAL(26); SB(); mf<false>(cacc[2], cA[1][0], cB[1][2]); RCA(2, 0); SB();
  mf<false>(cacc[3], cA[1][0], cB[1][3]); SB(); mf<false>(cacc[4], cA[1][0], cB[1][4]); RCB(2, 0); SB(); mf<false>(cacc[5], cA[1][0], cB[1][5]);
  WAL(27); SB(); mf<false>(cacc[6], cA[1][0], cB[1][6]); RCB(2, 1); SB(); mf<false>(cacc[7], cA[1][0], cB[1][7]); SB();
  mf<false>(cacc[1], cA[1][1], cB[1][0]); RCB(2, 2); SB(); mf<false>(cacc[2], cA[1][1], cB[1][1]); SB(); mf<false>(cacc[3], cA[1][1], cB[1][2]);
  RCB(2, 3); WAL(28); SB(); mf<false>(cacc[4], cA[1][1], cB[1][3]); SB(); mf<false>(cacc[5], cA[1][1], cB[1][4]); RCB(2, 4); SB();
  mf<false>(cacc[6], cA[1][1], cB[1][5]); SB(); mf<false>(cacc[7], cA[1][1], cB[1][6]); RCB(2, 5); WAL(29); SB();
  mf<false>(cacc[2], cA[1][2], cB[1][0]); SB(); mf<false>(cacc[3], cA[1][2], cB[1][1]); RCB(2, 6); SB(); mf<false>(cacc[4], cA[1][2], cB[1][2]); SB();
  mf<false>(cacc[5], cA[1][2], cB[1][3]); RCB(2, 7); SB(); mf<false>(cacc[6], cA[1][2], cB[1][4]); WAL(30); SB();
  mf<false>(cacc[7], cA[1][2], cB[1][5]); RCA(2, 1); SB(); mf<false>(cacc[3], cA[1][3], cB[1][0]); SB(); mf<false>(cacc[4], cA[1][3], cB[1][1]);
  RCA(2, 2); SB(); mf<false>(cacc[5], cA[1][3], cB[1][2]); WAL(31); SB(); mf<false>(cacc[6], cA[1][3], cB[1][3]); RCA(2, 3); SB();
  mf<false>(cacc[7], cA[1][3], cB[1][4]); SB(); mf<false>(cacc[4], cA[1][4], cB[1][0]); RCA(2, 4); SB(); mf<false>(cacc[5], cA[1][4], cB[1][1]); SB();
  mf<false>(cacc[6], cA[1][4], cB[1][2]); RCA(2, 5); WAL(32); SB(); mf<false>(cacc[7], cA[1][4], cB[1][3]); SB();
  mf<false>(cacc[5], cA[1][5], cB[1][0]); RCA(2, 6); SB(); mf<false>(cacc[6], cA[1][5], cB[1][1]); SB(); mf<false>(cacc[7], cA[1][5], cB[1][2]);
  RCA(2, 7); WAL(33); SB(); mf<false>(cacc[6], cA[1][6], cB[1][0]); SB(); mf<false>(cacc[7], cA[1][6], cB[1][1]); SB();
  mf<false>(cacc[7], cA[1][7], cB[1][0]); SB();
  mf<false>(cacc[0], cA[0][0], cB[0][0]); SB(); mf<false>(cacc[1], cA[0][0], cB[0][1]); WAL(34); SB(); mf<false>(cacc[2], cA[0][0], cB[0][2]);
  RCA(3, 0); SB(); mf<false>(cacc[3], cA[0][0], cB[0][3]); SB(); mf<false>(cacc[4], cA[0][0], cB[0][4]); RCB(3, 0); SB();
  mf<false>(cacc[5], cA[0][0], cB[0][5]); WAL(35); SB(); mf<false>(cacc[6], cA[0][0], cB[0][6]); RCB(3, 1); SB();
  mf<false>(cacc[7], cA[0][0], cB[0][7]); SB(); mf<false>(cacc[1], cA[0][1], cB[0][0]); RCB(3, 2); SB(); mf<false>(cacc[2], cA[0][1], cB[0][1]); SB();
  mf<false>(cacc[3], cA[0][1], cB[0][2]); RCB(3, 3); WAL(36); SB(); mf<false>(cacc[4], cA[0][1], cB[0][3]); SB();
  mf<false>(cacc[5], cA[0][1], cB[0][4]); RCB(3, 4); SB(); mf<false>(cacc[6], cA[0][1], cB[0][5]); SB(); mf<false>(cacc[7], cA[0][1], cB[0][6]);
  RCB(3, 5); WAL(37); SB(); mf<false>(cacc[2], cA[0][2], cB[0][0]); SB(); mf<false>(cacc[3], cA[0][2], cB[0][1]); RCB(3, 6); SB();
  mf<false>(cacc[4], cA[0][2], cB[0][2]); SB(); mf<false>(cacc[5], cA[0][2], cB[0][3]); RCB(3, 7); SB(); mf<false>(cacc[6], cA[0][2], cB[0][4]);
  WAL(38); SB(); mf<false>(cacc[7], cA[0][2], cB[0][5]); RCA(3, 1); SB(); mf<false>(cacc[3], cA[0][3], cB[0][0]); SB();
  mf<false>(cacc[4], cA[0][3], cB[0][1]); RCA(3, 2); SB(); mf<false>(cacc[5], cA[0][3], cB[0][2]); WAL(39); SB();
  mf<false>(cacc[6], cA[0][3], cB[0][3]); RCA(3, 3); SB(); mf<false>(cacc[7], cA[0][3], cB[0][4]); SB(); mf<false>(cacc[4], cA[0][4], cB[0][0]);
  RCA(3, 4); SB(); mf<false>(cacc[5], cA[0][4], cB[0][1]); SB(); mf<false>(cacc[6], cA[0][4], cB[0][2]); RCA(3, 5); WAL(40); SB();
  mf<false>(cacc[7], cA[0][4], cB[0][3]); SB(); mf<false>(cacc[5], cA[0][5], cB[0][0]); RCA(3, 6); SB(); mf<false>(cacc[6], cA[0][5], cB[0][1]); SB();
  mf<false>(cacc[7], cA[0][5], cB[0][2]); RCA(3, 7); WAL(41); SB(); mf<false>(cacc[6], cA[0][6], cB[0][0]); SB();
  mf<false>(cacc[7], cA[0][6], cB[0][1]); SB(); mf<false>(cacc[7], cA[0][7], cB[0][0]); SB();
  mf<false>(cacc[0], cA[1][0], cB[1][0]); SB(); mf<false>(cacc[1], cA[1][0], cB[1][1]); WAL(42); SB(); mf<false>(cacc[2], cA[1][0], cB[1][2]); SB();
  mf<false>(cacc[3], cA[1][0], cB[1][3]); SB(); mf<false>(cacc[4], cA[1][0], cB[1][4]); SB(); mf<false>(cacc[5], cA[1][0], cB[1][5]); WAL(43); SB();
  mf<false>(cacc[6], cA[1][0], cB[1][6]); SB(); mf<false>(cacc[7], cA[1][0], cB[1][7]); SB(); mf<false>(cacc[1], cA[1][1], cB[1][0]); SB();
  mf<false>(cacc[2], cA[1][1], cB[1][1]); WAL(44); CPW(0); SB(); mf<false>(cacc[3], cA[1][1], cB[1][2]); SB(); mf<false>(cacc[4], cA[1][1], cB[1][3]);
  SB(); mf<false>(cacc[5], cA[1][1], cB[1][4]); SB(); mf<false>(cacc[6], cA[1][1], cB[1][5]); WAL(45); SB(); mf<false>(cacc[7], cA[1][1], cB[1][6]);
  SB(); mf<false>(cacc[2], cA[1][2], cB[1][0]); SB(); mf<false>(cacc[3], cA[1][2], cB[1][1]); SB(); mf<false>(cacc[4], cA[1][2], cB[1][2]); CPW(1);
  SB(); mf<false>(cacc[5], cA[1][2], cB[1][3]); WAL(46); SB(); mf<false>(cacc[6], cA[1][2], cB[1][4]); SB(); mf<false>(cacc[7], cA[1][2], cB[1][5]);
  SB(); mf<false>(cacc[3], cA[1][3], cB[1][0]); SB(); mf<false>(cacc[4], cA[1][3], cB[1][1]); WAL(47); SB(); mf<false>(cacc[5], cA[1][3], cB[1][2]);
  SB(); mf<false>(cacc[6], cA[1][3], cB[1][3]); CPW(2); SB(); mf<false>(cacc[7], cA[1][3], cB[1][4]); SB(); mf<false>(cacc[4], cA[1][4], cB[1][0]);
  WAL(48); SB(); mf<false>(cacc[5], cA[1][4], cB[1][1]); SB(); mf<false>(cacc[6], cA[1][4], cB[1][2]); SB(); mf<false>(cacc[7], cA[1][4], cB[1][3]);
  SB(); mf<false>(cacc[5], cA[1][5], cB[1][0]); WAL(49); CPW(3); SB(); mf<false>(cacc[6], cA[1][5], cB[1][1]); SB();
  mf<false>(cacc[7], cA[1][5], cB[1][2]); SB(); mf<false>(cacc[6], cA[1][6], cB[1][0]); SB(); mf<false>(cacc[7], cA[1][6], cB[1][1]);
  ld8c(fl, (l & 15) * 8, rWS, WS_FH + (rt >> 1) * 128); SB(); mf<false>(cacc[7], cA[1][7], cB[1][0]); CPW(4); SB();
  mfdone();
  WAL(50); WAL(51); WAL(52);
  STAMP(8);
  {
    UNR for (int i = 5; i < 8; ++i) CPW(i);
    WAL(53); WAL(54); WAL(55);
    lbar();
    waitv<6>(); TIE1(fl); nrdy = __builtin_amdgcn_ballot_w64(fl.x != e0 || fl.y != e1); ld8c(fl2, (l & 15) * 8, rWS, WS_FH + (rt >> 1) * 128); HBEA();
    const float dd = blo(dv);
    LAS char* pr = lds + L_RED + chw * 16384 + hw * 4096 + (((2 * l) ^ ((l >> 2) & 3)) * 16);
    LAS char* pr1 = lds + L_RED + chw * 16384 + hw * 4096 + (((2 * l + 1) ^ ((l >> 2) & 3)) * 16);
    UNR for (int p = 0; p < 2; ++p) {
      const f32x4 s0 = *(LAS f32x4*)(pr + p * 2048) + *(LAS f32x4*)(pr + p * 2048 + 8192);
      const f32x4 s1 = *(LAS f32x4*)(pr1 + p * 2048) + *(LAS f32x4*)(pr1 + p * 2048 + 8192);
      const u32x4 uu = *(LAS u32x4*)(lds + L_R + chw * R_CH + (2040 - 1024 * hw - 512 * p - 8 * l) * 2);
      u32x4 y;
      y.x = pk2(rnd(f32x2{s0[0], s0[1]}) + rnd(f32x2{bhi(uu.w), blo(uu.w)} * dd));
      y.y = pk2(rnd(f32x2{s0[2], s0[3]}) + rnd(f32x2{bhi(uu.z), blo(uu.z)} * dd));
      y.z = pk2(rnd(f32x2{s1[0], s1[1]}) + rnd(f32x2{bhi(uu.y), blo(uu.y)} * dd));
      y.w = pk2(rnd(f32x2{s1[2], s1[3]}) + rnd(f32x2{bhi(uu.x), blo(uu.x)} * dd));
      st16c<0>(y, l * 16, rWS, WS_Y2 + (ch0 + chw) * 4096 + (1024 * hw + 512 * p) * 2);
    }
  }
  if (nrdy) { asm volatile("s_waitcnt vmcnt(2) ; PB_BEGIN" ::: "memory"); poll(fl2, (l & 15) * 8, rWS, WS_FH + (rt >> 1) * 128, e0, e1); asm volatile("; PB_END"); }
  HBEB();
  STAMP(9);

  f32x4 acc2[4][4];
  LAS char* pr2[3]; LAS char* py = opq(lds + L_Y2 + (tid >> 3) * 160 + (tid & 7) * 16);
  UNR for (int b = 1; b < 4; ++b) pr2[b - 1] = opq(lds + L_RED2 + (((w + b) & 3) * 3 + b - 1) * 4096 + l * 16);
#define R2W(a, b) *(LAS f32x4*)(pr2[(b) - 1] + (a) * 1024) = acc2[a][b]
#define YTW() TIE1(yt[0]); TIE1(yt[1]); *(LAS u32x4*)py = yt[0]; *(LAS u32x4*)(py + 5120) = yt[1]
  HBL(8); HBL(9); HBL(10); HBL(11); HBL(12); HBL(13); HBL(14); HBL(15);
  waitv<12>(); TIE4(hb[0][0], hb[0][1], hb[0][2], hb[0][3]); TIE4A(wa[0][0], wa[0][1], wa[0][2], wa[0][3]); TIE1(fl2);
  mfa<true>(acc2[0][1], wa[0][0], hb[0][1]); SB(); mfa<true>(acc2[1][1], wa[0][1], hb[0][1]); SB(); mfa<true>(acc2[2][1], wa[0][2], hb[0][1]); SB();
  mfa<true>(acc2[3][1], wa[0][3], hb[0][1]); HBL(17); SB(); mfa<true>(acc2[0][2], wa[0][0], hb[0][2]); SB();
  mfa<true>(acc2[1][2], wa[0][1], hb[0][2]); SB(); mfa<true>(acc2[2][2], wa[0][2], hb[0][2]); SB(); mfa<true>(acc2[3][2], wa[0][3], hb[0][2]);
  HBL(18); SB(); mfa<true>(acc2[0][3], wa[0][0], hb[0][3]); SB(); mfa<true>(acc2[1][3], wa[0][1], hb[0][3]); SB();
  mfa<true>(acc2[2][3], wa[0][2], hb[0][3]); SB(); mfa<true>(acc2[3][3], wa[0][3], hb[0][3]); HBL(19); SB();
  mfa<true>(acc2[0][0], wa[0][0], hb[0][0]); SB(); mfa<true>(acc2[1][0], wa[0][1], hb[0][0]); SB(); mfa<true>(acc2[2][0], wa[0][2], hb[0][0]); SB();
  mfa<true>(acc2[3][0], wa[0][3], hb[0][0]); HBL(16); SB();
  waitv_bar<12, 99>(); st8c<0>(u32x2{e0, e1}, vone, rWS, WS_F3 + (cg * 32 + rt) * 8);
  TIE4(hb[1][0], hb[1][1], hb[1][2], hb[1][3]); TIE4A(wa[1][0], wa[1][1], wa[1][2], wa[1][3]); mfa<false>(acc2[0][1], wa[1][0], hb[1][1]); SB();
  mfa<false>(acc2[1][1], wa[1][1], hb[1][1]); SB(); mfa<false>(acc2[2][1], wa[1][2], hb[1][1]); SB(); mfa<false>(acc2[3][1], wa[1][3], hb[1][1]);
  HBL(21); SB(); mfa<false>(acc2[0][2], wa[1][0], hb[1][2]); SB(); mfa<false>(acc2[1][2], wa[1][1], hb[1][2]); SB();
  mfa<false>(acc2[2][2], wa[1][2], hb[1][2]); SB(); mfa<false>(acc2[3][2], wa[1][3], hb[1][2]); HBL(22); SB();
  mfa<false>(acc2[0][3], wa[1][0], hb[1][3]); SB(); mfa<false>(acc2[1][3], wa[1][1], hb[1][3]); SB(); mfa<false>(acc2[2][3], wa[1][2], hb[1][3]);
  SB(); mfa<false>(acc2[3][3], wa[1][3], hb[1][3]); HBL(23); SB(); mfa<false>(acc2[0][0], wa[1][0], hb[1][0]); SB();
  mfa<false>(acc2[1][0], wa[1][1], hb[1][0]); SB(); mfa<false>(acc2[2][0], wa[1][2], hb[1][0]); SB(); mfa<false>(acc2[3][0], wa[1][3], hb[1][0]);
  HBL(20); SB();
  waitv<13>(); TIE4(hb[2][0], hb[2][1], hb[2][2], hb[2][3]); TIE4A(wa[2][0], wa[2][1], wa[2][2], wa[2][3]);
  mfa<false>(acc2[0][1], wa[2][0], hb[2][1]); SB(); mfa<false>(acc2[1][1], wa[2][1], hb[2][1]); SB(); mfa<false>(acc2[2][1], wa[2][2], hb[2][1]);
  SB(); mfa<false>(acc2[3][1], wa[2][3], hb[2][1]); HBL(25); SB(); mfa<false>(acc2[0][2], wa[2][0], hb[2][2]); SB();
  mfa<false>(acc2[1][2], wa[2][1], hb[2][2]); SB(); mfa<false>(acc2[2][2], wa[2][2], hb[2][2]); SB(); mfa<false>(acc2[3][2], wa[2][3], hb[2][2]);
  HBL(26); SB(); mfa<false>(acc2[0][3], wa[2][0], hb[2][3]); SB(); mfa<false>(acc2[1][3], wa[2][1], hb[2][3]); SB();
  mfa<false>(acc2[2][3], wa[2][2], hb[2][3]); SB(); mfa<false>(acc2[3][3], wa[2][3], hb[2][3]); HBL(27); SB();
  mfa<false>(acc2[0][0], wa[2][0], hb[2][0]); SB(); mfa<false>(acc2[1][0], wa[2][1], hb[2][0]); SB(); mfa<false>(acc2[2][0], wa[2][2], hb[2][0]);
  SB(); mfa<false>(acc2[3][0], wa[2][3], hb[2][0]); HBL(24); SB();
  waitv<13>(); TIE4(hb[3][0], hb[3][1], hb[3][2], hb[3][3]); TIE4A(wa[3][0], wa[3][1], wa[3][2], wa[3][3]);
  mfa<false>(acc2[0][1], wa[3][0], hb[3][1]); SB(); mfa<false>(acc2[1][1], wa[3][1], hb[3][1]); SB(); mfa<false>(acc2[2][1], wa[3][2], hb[3][1]);
  SB(); mfa<false>(acc2[3][1], wa[3][3], hb[3][1]); HBL(29); SB(); mfa<false>(acc2[0][2], wa[3][0], hb[3][2]); SB();
  mfa<false>(acc2[1][2], wa[3][1], hb[3][2]); SB(); mfa<false>(acc2[2][2], wa[3][2], hb[3][2]); SB(); mfa<false>(acc2[3][2], wa[3][3], hb[3][2]);
  HBL(30); SB(); mfa<false>(acc2[0][3], wa[3][0], hb[3][3]); SB(); mfa<false>(acc2[1][3], wa[3][1], hb[3][3]); SB();
  mfa<false>(acc2[2][3], wa[3][2], hb[3][3]); SB(); mfa<false>(acc2[3][3], wa[3][3], hb[3][3]); HBL(31); SB();
  mfa<false>(acc2[0][0], wa[3][0], hb[3][0]); SB(); mfa<false>(acc2[1][0], wa[3][1], hb[3][0]); SB(); mfa<false>(acc2[2][0], wa[3][2], hb[3][0]);
  SB(); mfa<false>(acc2[3][0], wa[3][3], hb[3][0]); HBL(28); SB();
  waitv<13>(); TIE4(hb[0][0], hb[0][1], hb[0][2], hb[0][3]); TIE4A(wa[4][0], wa[4][1], wa[4][2], wa[4][3]);
  mfa<false>(acc2[0][1], wa[4][0], hb[0][1]); SB(); mfa<false>(acc2[1][1], wa[4][1], hb[0][1]); SB(); mfa<false>(acc2[2][1], wa[4][2], hb[0][1]);
  SB(); mfa<false>(acc2[3][1], wa[4][3], hb[0][1]); HBL(33); SB(); mfa<false>(acc2[0][2], wa[4][0], hb[0][2]); SB();
  mfa<false>(acc2[1][2], wa[4][1], hb[0][2]); SB(); mfa<false>(acc2[2][2], wa[4][2], hb[0][2]); SB(); mfa<false>(acc2[3][2], wa[4][3], hb[0][2]);
  HBL(34); SB(); mfa<false>(acc2[0][3], wa[4][0], hb[0][3]); SB(); mfa<false>(acc2[1][3], wa[4][1], hb[0][3]); SB();
  mfa<false>(acc2[2][3], wa[4][2], hb[0][3]); SB(); mfa<false>(acc2[3][3], wa[4][3], hb[0][3]); HBL(35); SB();
  mfa<false>(acc2[0][0], wa[4][0], hb[0][0]); SB(); mfa<false>(acc2[1][0], wa[4][1], hb[0][0]); SB(); mfa<false>(acc2[2][0], wa[4][2], hb[0][0]);
  SB(); mfa<false>(acc2[3][0], wa[4][3], hb[0][0]); HBL(32); SB();
  waitv<12>(); TIE4(hb[1][0], hb[1][1], hb[1][2], hb[1][3]); TIE4A(wa[5][0], wa[5][1], wa[5][2], wa[5][3]);
  mfa<false>(acc2[0][1], wa[5][0], hb[1][1]); SB(); mfa<false>(acc2[1][1], wa[5][1], hb[1][1]); SB(); mfa<false>(acc2[2][1], wa[5][2], hb[1][1]);
  SB(); mfa<false>(acc2[3][1], wa[5][3], hb[1][1]); HBL(37); SB(); mfa<false>(acc2[0][2], wa[5][0], hb[1][2]); SB();
  mfa<false>(acc2[1][2], wa[5][1], hb[1][2]); SB(); mfa<false>(acc2[2][2], wa[5][2], hb[1][2]); SB(); mfa<false>(acc2[3][2], wa[5][3], hb[1][2]);
  HBL(38); SB(); mfa<false>(acc2[0][3], wa[5][0], hb[1][3]); SB(); mfa<false>(acc2[1][3], wa[5][1], hb[1][3]); SB();
  mfa<false>(acc2[2][3], wa[5][2], hb[1][3]); SB(); mfa<false>(acc2[3][3], wa[5][3], hb[1][3]); HBL(39); SB();
  mfa<false>(acc2[0][0], wa[5][0], hb[1][0]); SB(); mfa<false>(acc2[1][0], wa[5][1], hb[1][0]); SB(); mfa<false>(acc2[2][0], wa[5][2], hb[1][0]);
  SB(); mfa<false>(acc2[3][0], wa[5][3], hb[1][0]); HBL(36); SB();
  waitv<12>(); TIE4(hb[2][0], hb[2][1], hb[2][2], hb[2][3]); TIE4A(wa[6][0], wa[6][1], wa[6][2], wa[6][3]);
  mfa<false>(acc2[0][1], wa[6][0], hb[2][1]); SB(); mfa<false>(acc2[1][1], wa[6][1], hb[2][1]); SB(); mfa<false>(acc2[2][1], wa[6][2], hb[2][1]);
  SB(); mfa<false>(acc2[3][1], wa[6][3], hb[2][1]); HBL(41); SB(); mfa<false>(acc2[0][2], wa[6][0], hb[2][2]); SB();
  mfa<false>(acc2[1][2], wa[6][1], hb[2][2]); SB(); mfa<false>(acc2[2][2], wa[6][2], hb[2][2]); SB(); mfa<false>(acc2[3][2], wa[6][3], hb[2][2]);
  HBL(42); SB(); mfa<false>(acc2[0][3], wa[6][0], hb[2][3]); SB(); mfa<false>(acc2[1][3], wa[6][1], hb[2][3]); SB();
  mfa<false>(acc2[2][3], wa[6][2], hb[2][3]); SB(); mfa<false>(acc2[3][3], wa[6][3], hb[2][3]); HBL(43); SB();
  mfa<false>(acc2[0][0], wa[6][0], hb[2][0]); SB(); mfa<false>(acc2[1][0], wa[6][1], hb[2][0]); SB(); mfa<false>(acc2[2][0], wa[6][2], hb[2][0]);
  SB(); mfa<false>(acc2[3][0], wa[6][3], hb[2][0]); HBL(40); SB();
  waitv<12>(); TIE4(hb[3][0], hb[3][1], hb[3][2], hb[3][3]); TIE4A(wa[7][0], wa[7][1], wa[7][2], wa[7][3]);
  mfa<false>(acc2[0][1], wa[7][0], hb[3][1]); ld8c(fl, (l & 31) * 8, rWS, WS_F3 + cg * 256); SB(); mfa<false>(acc2[1][1], wa[7][1], hb[3][1]); SB();
  mfa<false>(acc2[2][1], wa[7][2], hb[3][1]); SB(); mfa<false>(acc2[3][1], wa[7][3], hb[3][1]); HBL(45); SB();
  mfa<false>(acc2[0][2], wa[7][0], hb[3][2]); SB(); mfa<false>(acc2[1][2], wa[7][1], hb[3][2]); SB(); mfa<false>(acc2[2][2], wa[7][2], hb[3][2]);
  SB(); mfa<false>(acc2[3][2], wa[7][3], hb[3][2]); HBL(46); SB(); mfa<false>(acc2[0][3], wa[7][0], hb[3][3]); SB();
  mfa<false>(acc2[1][3], wa[7][1], hb[3][3]); SB(); mfa<false>(acc2[2][3], wa[7][2], hb[3][3]); SB(); mfa<false>(acc2[3][3], wa[7][3], hb[3][3]);
  HBL(47); SB(); mfa<false>(acc2[0][0], wa[7][0], hb[3][0]); SB(); mfa<false>(acc2[1][0], wa[7][1], hb[3][0]); SB();
  mfa<false>(acc2[2][0], wa[7][2], hb[3][0]); SB(); mfa<false>(acc2[3][0], wa[7][3], hb[3][0]); HBL(44); SB();
  waitv<13>(); TIE4(hb[0][0], hb[0][1], hb[0][2], hb[0][3]); TIE4A(wa[8][0], wa[8][1], wa[8][2], wa[8][3]);
  mfa<false>(acc2[0][1], wa[8][0], hb[0][1]); SB(); mfa<false>(acc2[1][1], wa[8][1], hb[0][1]); SB(); mfa<false>(acc2[2][1], wa[8][2], hb[0][1]);
  SB(); mfa<false>(acc2[3][1], wa[8][3], hb[0][1]); HBL(49); SB(); mfa<false>(acc2[0][2], wa[8][0], hb[0][2]); SB();
  mfa<false>(acc2[1][2], wa[8][1], hb[0][2]); SB(); mfa<false>(acc2[2][2], wa[8][2], hb[0][2]); SB(); mfa<false>(acc2[3][2], wa[8][3], hb[0][2]);
  HBL(50); SB(); mfa<false>(acc2[0][3], wa[8][0], hb[0][3]); SB(); mfa<false>(acc2[1][3], wa[8][1], hb[0][3]); SB();
  mfa<false>(acc2[2][3], wa[8][2], hb[0][3]); SB(); mfa<false>(acc2[3][3], wa[8][3], hb[0][3]); HBL(51); SB();
  mfa<false>(acc2[0][0], wa[8][0], hb[0][0]); SB(); mfa<false>(acc2[1][0], wa[8][1], hb[0][0]); SB(); mfa<false>(acc2[2][0], wa[8][2], hb[0][0]);
  SB(); mfa<false>(acc2[3][0], wa[8][3], hb[0][0]); HBL(48); SB();
  waitv<13>(); TIE4(hb[1][0], hb[1][1], hb[1][2], hb[1][3]); TIE4A(wa[9][0], wa[9][1], wa[9][2], wa[9][3]);
  mfa<false>(acc2[0][1], wa[9][0], hb[1][1]); SB(); mfa<false>(acc2[1][1], wa[9][1], hb[1][1]); SB(); mfa<false>(acc2[2][1], wa[9][2], hb[1][1]);
  SB(); mfa<false>(acc2[3][1], wa[9][3], hb[1][1]); HBL(53); SB(); mfa<false>(acc2[0][2], wa[9][0], hb[1][2]); SB();
  mfa<false>(acc2[1][2], wa[9][1], hb[1][2]); SB(); mfa<false>(acc2[2][2], wa[9][2], hb[1][2]); SB(); mfa<false>(acc2[3][2], wa[9][3], hb[1][2]);
  HBL(54); SB(); mfa<false>(acc2[0][3], wa[9][0], hb[1][3]); SB(); mfa<false>(acc2[1][3], wa[9][1], hb[1][3]); SB();
  mfa<false>(acc2[2][3], wa[9][2], hb[1][3]); SB(); mfa<false>(acc2[3][3], wa[9][3], hb[1][3]); HBL(55); SB();
  mfa<false>(acc2[0][0], wa[9][0], hb[1][0]); SB(); mfa<false>(acc2[1][0], wa[9][1], hb[1][0]); SB(); mfa<false>(acc2[2][0], wa[9][2], hb[1][0]);
  SB(); mfa<false>(acc2[3][0], wa[9][3], hb[1][0]); HBL(52); SB();
  waitv<12>(); TIE4(hb[2][0], hb[2][1], hb[2][2], hb[2][3]); TIE4A(wa[10][0], wa[10][1], wa[10][2], wa[10][3]);
  poll(fl, (l & 31) * 8, rWS, WS_F3 + cg * 256, e0, e1); mfa<false>(acc2[0][1], wa[10][0], hb[2][1]);
  ld16c<0>(yt[0], (tid >> 3) * 4096 + (tid & 7) * 16, rWS, WS_Y2 + cg * 64 * 4096 + rt * 128 + 0); SB(); mfa<false>(acc2[1][1], wa[10][1], hb[2][1]);
  ld16c<0>(yt[1], (tid >> 3) * 4096 + (tid & 7) * 16, rWS, WS_Y2 + cg * 64 * 4096 + rt * 128 + 131072);
  ld8<0>(qv[0], (16 * w + r16) * 1024 + 8 * g, rQ, rt * 65536 + cg * 128); SB(); mfa<false>(acc2[2][1], wa[10][2], hb[2][1]); SB();
  mfa<false>(acc2[3][1], wa[10][3], hb[2][1]); SB(); mfa<false>(acc2[0][2], wa[10][0], hb[2][2]);
  ld8<32>(qv[1], (16 * w + r16) * 1024 + 8 * g, rQ, rt * 65536 + cg * 128); SB(); mfa<false>(acc2[1][2], wa[10][1], hb[2][2]); SB();
  mfa<false>(acc2[2][2], wa[10][2], hb[2][2]); SB(); mfa<false>(acc2[3][2], wa[10][3], hb[2][2]);
  ld8<64>(qv[2], (16 * w + r16) * 1024 + 8 * g, rQ, rt * 65536 + cg * 128); SB(); mfa<false>(acc2[0][3], wa[10][0], hb[2][3]); SB();
  mfa<false>(acc2[1][3], wa[10][1], hb[2][3]); SB(); mfa<false>(acc2[2][3], wa[10][2], hb[2][3]);
  ld8<96>(qv[3], (16 * w + r16) * 1024 + 8 * g, rQ, rt * 65536 + cg * 128); SB(); mfa<false>(acc2[3][3], wa[10][3], hb[2][3]); SB();
  mfa<false>(acc2[0][0], wa[10][0], hb[2][0]); SB(); mfa<false>(acc2[1][0], wa[10][1], hb[2][0]);
  ld4c(gg, r16 * 4, rWS, WS_GATE + (64 * rt + 16 * w) * 4); SB(); mfa<false>(acc2[2][0], wa[10][2], hb[2][0]); SB();
  mfa<false>(acc2[3][0], wa[10][3], hb[2][0]); SB();
  waitv<15>(); TIE4(hb[3][0], hb[3][1], hb[3][2], hb[3][3]); TIE4A(wa[11][0], wa[11][1], wa[11][2], wa[11][3]);
  mfa<false>(acc2[0][1], wa[11][0], hb[3][1]); SB(); mfa<false>(acc2[1][1], wa[11][1], hb[3][1]); SB(); mfa<false>(acc2[2][1], wa[11][2], hb[3][1]);
  SB(); mfa<false>(acc2[3][1], wa[11][3], hb[3][1]); SB(); mfa<false>(acc2[0][2], wa[11][0], hb[3][2]); SB();
  mfa<false>(acc2[1][2], wa[11][1], hb[3][2]); SB(); mfa<false>(acc2[2][2], wa[11][2], hb[3][2]); SB(); mfa<false>(acc2[3][2], wa[11][3], hb[3][2]);
  SB(); mfa<false>(acc2[0][3], wa[11][0], hb[3][3]); SB(); mfa<false>(acc2[1][3], wa[11][1], hb[3][3]); SB();
  mfa<false>(acc2[2][3], wa[11][2], hb[3][3]); SB(); mfa<false>(acc2[3][3], wa[11][3], hb[3][3]); SB(); mfa<false>(acc2[0][0], wa[11][0], hb[3][0]);
  SB(); mfa<false>(acc2[1][0], wa[11][1], hb[3][0]); SB(); mfa<false>(acc2[2][0], wa[11][2], hb[3][0]); SB();
  mfa<false>(acc2[3][0], wa[11][3], hb[3][0]); SB();
  waitv<7>(); TIE4(hb[0][0], hb[0][1], hb[0][2], hb[0][3]); TIE4A(wa[12][0], wa[12][1], wa[12][2], wa[12][3]);
  TIE4(hb[1][0], hb[1][1], hb[1][2], hb[1][3]); TIE4A(wa[13][0], wa[13][1], wa[13][2], wa[13][3]); mfa<false>(acc2[0][1], wa[12][0], hb[0][1]); SB();
  mfa<false>(acc2[1][1], wa[12][1], hb[0][1]); SB(); mfa<false>(acc2[2][1], wa[12][2], hb[0][1]); SB(); mfa<false>(acc2[3][1], wa[12][3], hb[0][1]);
  SB(); mfa<false>(acc2[0][1], wa[13][0], hb[1][1]); SB(); mfa<false>(acc2[1][1], wa[13][1], hb[1][1]); SB();
  mfa<false>(acc2[2][1], wa[13][2], hb[1][1]); SB(); mfa<false>(acc2[3][1], wa[13][3], hb[1][1]); SB(); mfa<false>(acc2[0][2], wa[12][0], hb[0][2]);
  SB(); mfa<false>(acc2[1][2], wa[12][1], hb[0][2]); SB(); mfa<false>(acc2[2][2], wa[12][2], hb[0][2]); SB();
  mfa<false>(acc2[3][2], wa[12][3], hb[0][2]); SB(); mfa<false>(acc2[0][2], wa[13][0], hb[1][2]); SB(); mfa<false>(acc2[1][2], wa[13][1], hb[1][2]);
  R2W(0, 1); SB(); mfa<false>(acc2[2][2], wa[13][2], hb[1][2]); R2W(1, 1); SB(); mfa<false>(acc2[3][2], wa[13][3], hb[1][2]); R2W(2, 1); SB();
  mfa<false>(acc2[0][3], wa[12][0], hb[0][3]); R2W(3, 1); SB(); mfa<false>(acc2[1][3], wa[12][1], hb[0][3]); SB();
  mfa<false>(acc2[2][3], wa[12][2], hb[0][3]); SB(); mfa<false>(acc2[3][3], wa[12][3], hb[0][3]); waitv<5>(); YTW(); SB();
  mfa<false>(acc2[0][3], wa[13][0], hb[1][3]); SB(); mfa<false>(acc2[1][3], wa[13][1], hb[1][3]); R2W(0, 2); SB();
  mfa<false>(acc2[2][3], wa[13][2], hb[1][3]); R2W(1, 2); SB(); mfa<false>(acc2[3][3], wa[13][3], hb[1][3]); R2W(2, 2); SB();
  mfa<false>(acc2[0][0], wa[12][0], hb[0][0]); R2W(3, 2); SB(); mfa<false>(acc2[1][0], wa[12][1], hb[0][0]); SB();
  mfa<false>(acc2[2][0], wa[12][2], hb[0][0]); SB(); mfa<false>(acc2[3][0], wa[12][3], hb[0][0]); SB(); mfa<false>(acc2[0][0], wa[13][0], hb[1][0]);
  SB(); mfa<false>(acc2[1][0], wa[13][1], hb[1][0]); R2W(0, 3); SB(); mfa<false>(acc2[2][0], wa[13][2], hb[1][0]); R2W(1, 3); SB();
  mfa<false>(acc2[3][0], wa[13][3], hb[1][0]); R2W(2, 3); SB();
  mfdone();
  STAMP(10);
  R2W(3, 3);
  waitv<0>(); TIE4(qv[0], qv[1], qv[2], qv[3]); TIE1(gg);
  lbar();
  {
    const float gf = __uint_as_float(gg), og = blo(pk(1.f - gf, 0.f));
    u32x2 o[4];
    UNR for (int a = 0; a < 4; ++a) {
      f32x4 s = acc2[a][0];
      UNR for (int k = 0; k < 3; ++k) s += *(LAS f32x4*)(lds + L_RED2 + ((w * 3 + k) * 4 + a) * 1024 + l * 16);
      const u32x2 y = trread(lds + L_Y2 + (16 * a + 4 * g + (r16 >> 2)) * 160 + (16 * w + 4 * (r16 & 3)) * 2);
      o[a].x = pk2(rnd(gf * rnd(up(qv[a].x) * up(y.x))) + rnd(og * rnd(f32x2{s[0], s[1]})));
      o[a].y = pk2(rnd(gf * rnd(up(qv[a].y) * up(y.y))) + rnd(og * rnd(f32x2{s[2], s[3]})));
    }
    const rsrc_t rO = mk(out, 2u << 20);
    UNR for (int p = 0; p < 2; ++p) {
      auto s0 = __builtin_amdgcn_permlane16_swap(o[2 * p].x, o[2 * p + 1].x, false, false);
      auto s1 = __builtin_amdgcn_permlane16_swap(o[2 * p].y, o[2 * p + 1].y, false, false);
      st16<0>(u32x4{s0[0], s1[0], s0[1], s1[1]}, (16 * w + r16) * 1024 + 32 * (g & 1) + 16 * (g >> 1), rO, rt * 65536 + cg * 128 + p * 64);
    }
  }
  STAMP(11); STAMPC(15);
  asm volatile("Lgm_end:");
  if (bid == 0) {
    u32x2 f; ld8c(f, tid * 8 + WS_F1, rWS, Z); waitv<0>();
    poll(f, tid * 8 + WS_F1, rWS, Z, e0, e1);
    lbar();
    st8c<0>(u32x2{e0, e1}, vone, rWS, Z);
  }
}

extern "C" void kernel_launch(void* const* d_in, const int* in_sizes, int n_in, void* d_out, int out_size, void* d_ws,
                              size_t ws_size, hipStream_t stream) {
  (void)hipFuncSetAttribute((const void*)gmix_kernel, hipFuncAttributeMaxDynamicSharedMemorySize, (int)LDS_BYTES);
  gmix_kernel<<<256, 256, LDS_BYTES, stream>>>((const u16*)d_in[0], (const u16*)d_in[1], (const u16*)d_in[2], (const u16*)d_in[3],
      (const u16*)d_in[4], (const u16*)d_in[5], (const u16*)d_in[7], (const u16*)d_in[8], (const u16*)d_in[9], (u16*)d_out, (char*)d_ws);
}
```
